```python
import torch

def _softmax(x, axis=-1):
    return torch.softmax(x, dim=axis)
def _gelu(x):
    return torch.nn.functional.gelu(x, approximate="tanh")
def _silu(x):
    return torch.nn.functional.silu(x)
def _sigmoid(x):
    return torch.sigmoid(x)
def _layernorm(x, axis=-1, eps=1e-5):
    m = x.mean(dim=axis, keepdim=True)
    v = x.var(dim=axis, keepdim=True, unbiased=False)
    return (x - m) / torch.sqrt(v + eps)
def _scatter_add(tgt, idx, src, axis=0):
    return tgt.clone().index_add_(axis, idx, src)
def _segment_sum(src, seg, n):
    out = torch.zeros((n,) + src.shape[1:], dtype=src.dtype)
    return out.index_add_(0, seg, src)
def _pad(x, pw):
    flat = tuple(v for pair in reversed(pw) for v in pair)
    return torch.nn.functional.pad(x, flat)

kernel_name = 'standalone__gqa_softmax__near_ridge'

def setup_inputs():

    _w = (lambda a: (lambda t: t.to(torch.bfloat16) if t.is_floating_point() else t)(torch.as_tensor(a)))
    B, Hq, Hkv, Sq, Sk, D = 8, 32, 8, 144, 1024, 128
    return {
        "q": _w((lambda *s: torch.randn(*s, dtype=torch.bfloat16) * 0.8)(B, Hq, Sq, D)),
        "k": _w((lambda *s: torch.randn(*s, dtype=torch.bfloat16) * 0.8)(B, Hkv, Sk, D)),
        "v": _w((lambda *s: torch.randn(*s, dtype=torch.bfloat16) * 0.05)(B, Hkv, Sk, D)),
    }

def reference(q, k, v):
    B, Hq, Sq, D = q.shape
    Hkv = k.shape[1]
    rep = Hq // Hkv
    k = (lambda x, n, axis: torch.repeat_interleave(x, n, dim=axis))(k, rep, axis=1)
    v = (lambda x, n, axis: torch.repeat_interleave(x, n, dim=axis))(v, rep, axis=1)
    scale = D ** -0.5
    scores = torch.einsum("bhqd,bhkd->bhqk", q, k) * scale
    p = _softmax(scores, axis=-1)
    return torch.einsum("bhqk,bhkd->bhqd", p, v)
```

```cpp
#include <hip/hip_runtime.h>

__global__ __launch_bounds__(512, 2) void gqa_fwd(const void* q, const void* k, const void* v, void* o) {
  __shared__ __attribute__((aligned(1024))) char lds[163840];
  const unsigned ldsb = (unsigned)(size_t)lds;
  const unsigned wid = __builtin_amdgcn_readfirstlane(threadIdx.x >> 6);
  asm volatile(
  "s_load_dwordx8 s[36:43], %0, 0x0\n\t"
  "v_mbcnt_lo_u32_b32 v250, -1, 0\n\t"
  "s_lshl_b32 s49, %1, 3\n\t"
  "v_mbcnt_hi_u32_b32 v250, -1, v250\n\t"
  "s_and_b32 s49, s49, 56\n\t"
  "s_lshr_b32 s50, %1, 5\n\t"
  "v_bfe_u32 v241, v250, 2, 3\n\t"
  "s_add_u32 s47, s49, s50\n\t"
  "v_lshrrev_b32_e32 v240, 5, v250\n\t"
  "s_bfe_u32 s50, %1, 0x20003\n\t"
  "v_lshlrev_b32_e32 v242, 8, v241\n\t"
  "s_lshl2_add_u32 s48, s47, s50\n\t"
  "v_and_b32_e32 v239, 3, v250\n\t"
  "s_mul_i32 s51, s48, 0x9000\n\t"
  "v_lshl_add_u32 v242, v240, 6, v242\n\t"
  "s_lshl_b32 s52, %2, 13\n\t"
  "v_lshl_add_u32 v242, v239, 4, v242\n\t"
  "s_add_u32 s53, s51, s52\n\t"
  "v_add_u32_e32 v253, 0x400, v242\n\t"
  "s_add_u32 s55, s52, 0x20000\n\t"
  "v_add_u32_e32 v254, 0x80, v242\n\t"
  "s_lshl_b32 s52, %2, 12\n\t"
  "v_xor_b32_e32 v255, 32, v253\n\t"
  "s_lshl_b32 s54, s47, 18\n\t"
  "v_xor_b32_e32 v243, 32, v254\n\t"
  "s_sub_u32 s52, s52, 0x4000\n\t"
  "s_add_u32 s49, s51, 0x8000\n\t"
  "s_add_u32 s50, s52, 0x1c000\n\t"
  "s_cmp_lt_u32 %2, 4\n\t"
  "s_cselect_b32 s49, s53, s49\n\t"
  "s_cselect_b32 s50, s55, s50\n\t"
  "s_add_u32 m0, s50, %3\n\t"
  "s_waitcnt lgkmcnt(0)\n\t"
  "s_add_u32 s56, s36, s49\n\t"
  "s_addc_u32 s57, s37, 0\n\t"
  "s_sub_u32 s56, s56, 0x400\n\t"
  "s_subb_u32 s57, s57, 0\n\t"
  "global_load_lds_dwordx4 v253, s[56:57] offset:0 nt\n\t"
  "global_load_lds_dwordx4 v254, s[56:57] offset:1024 nt\n\t"
  "global_load_lds_dwordx4 v255, s[56:57] offset:2048 nt\n\t"
  "global_load_lds_dwordx4 v243, s[56:57] offset:3072 nt\n\t"
  "s_cmp_lt_u32 %2, 4\n\t"
  "s_cbranch_scc0 .Lhstart_%=\n\t"
  "s_add_u32 m0, m0, 0x1000\n\t"
  "s_add_u32 s56, s56, 0x1000\n\t"
  "s_addc_u32 s57, s57, 0\n\t"
  "global_load_lds_dwordx4 v253, s[56:57] offset:0 nt\n\t"
  "global_load_lds_dwordx4 v254, s[56:57] offset:1024 nt\n\t"
  "global_load_lds_dwordx4 v255, s[56:57] offset:2048 nt\n\t"
  "global_load_lds_dwordx4 v243, s[56:57] offset:3072 nt\n\t"
  "s_branch .Lmain_%=\n\t"
  ".Lhstart_%=:\n\t"
  "s_add_u32 s72, s38, s54\n\t"
  "s_addc_u32 s73, s39, 0\n\t"
  "s_sub_u32 s72, s72, 0x400\n\t"
  "s_subb_u32 s73, s73, 0\n\t"
  "s_and_b32 s73, s73, 0xffff\n\t"
  "s_mov_b32 s74, 0x40400\n\t"
  "s_mov_b32 s75, 0x20000\n\t"
  "s_sub_u32 m0, m0, 0x4000\n\t"
  "s_mov_b32 s62, s52\n\t"
  "buffer_load_dwordx4 v253, s[72:75], s62 offen offset:0 nt lds\n\t"
  "buffer_load_dwordx4 v254, s[72:75], s62 offen offset:1024 nt lds\n\t"
  "buffer_load_dwordx4 v255, s[72:75], s62 offen offset:2048 nt lds\n\t"
  "buffer_load_dwordx4 v243, s[72:75], s62 offen offset:3072 nt lds\n\t"
  "s_setprio 2\n\t"
  "s_sub_u32 s82, %2, 4\n\t"
  "s_lshr_b32 s81, s82, 1\n\t"
  "s_and_b32 s50, s82, 1\n\t"
  "s_add_u32 s76, s40, s54\n\t"
  "s_addc_u32 s77, s41, 0\n\t"
  "s_sub_u32 s76, s76, 0x400\n\t"
  "s_subb_u32 s77, s77, 0\n\t"
  "s_and_b32 s77, s77, 0xffff\n\t"
  "s_mov_b32 s78, 0x40400\n\t"
  "s_mov_b32 s79, 0x20000\n\t"
  "s_add_u32 s80, s52, %3\n\t"
  "s_mov_b32 s63, s52\n\t"
  "s_add_u32 s62, s52, 0x4000\n\t"
  "s_mov_b32 s61, 0\n\t"
  "v_and_b32_e32 v183, 16, v250\n\t"
  "v_lshlrev_b32_e32 v183, 1, v183\n\t"
  "v_xor_b32_e32 v186, v183, v253\n\t"
  "v_xor_b32_e32 v187, v183, v254\n\t"
  "s_add_u32 s58, s42, s51\n\t"
  "s_addc_u32 s59, s43, 0\n\t"
  "s_branch .Lhelper_%=\n\t"
  ".Lmain_%=:\n\t"
  "s_add_u32 s55, s55, %3\n\t"
  "s_add_u32 s58, s42, s53\n\t"
  "s_addc_u32 s59, s43, 0\n\t"
  "v_lshrrev_b32_e32 v251, 4, v250\n\t"
  "v_and_b32_e32 v252, 15, v250\n\t"
  "v_lshrrev_b32_e32 v241, 3, v252\n\t"
  "v_lshlrev_b32_e32 v242, 11, v241\n\t"
  "v_lshlrev_b32_e32 v241, 1, v241\n\t"
  "v_xor_b32_e32 v241, v241, v251\n\t"
  "v_lshl_add_u32 v242, v241, 4, v242\n\t"
  "v_and_b32_e32 v241, 7, v252\n\t"
  "v_lshl_add_u32 v242, v241, 6, v242\n\t"
  "v_add_u32_e32 v244, %3, v242\n\t"
  "v_add_u32_e32 v243, s55, v242\n\t"
  "v_add_u32_e32 v247, 0x18000, v244\n\t"
  "v_lshrrev_b32_e32 v241, 1, v251\n\t"
  "v_lshlrev_b32_e32 v242, 11, v241\n\t"
  "v_and_b32_e32 v241, 1, v251\n\t"
  "v_mul_u32_u24_e32 v241, 0x120, v241\n\t"
  "v_add_u32_e32 v242, v242, v241\n\t"
  "v_lshrrev_b32_e32 v241, 2, v252\n\t"
  "v_lshl_add_u32 v242, v241, 6, v242\n\t"
  "v_and_b32_e32 v241, 3, v252\n\t"
  "v_lshl_add_u32 v242, v241, 3, v242\n\t"
  "v_add_u32_e32 v242, 0x4000, v242\n\t"
  "v_add_u32_e32 v245, %3, v242\n\t"
  "v_xor_b32_e32 v246, 32, v245\n\t"
  "v_mov_b32_e32 v248, v245\n\t"
  "v_mov_b32_e32 v249, v246\n\t"
  "v_mov_b64_e32 v[0:1], 0\n\t"
  "v_mov_b64_e32 v[2:3], 0\n\t"
  "v_mov_b64_e32 v[4:5], 0\n\t"
  "v_mov_b64_e32 v[6:7], 0\n\t"
  "v_mov_b64_e32 v[8:9], 0\n\t"
  "v_mov_b64_e32 v[10:11], 0\n\t"
  "v_mov_b64_e32 v[12:13], 0\n\t"
  "v_mov_b64_e32 v[14:15], 0\n\t"
  "v_mov_b64_e32 v[16:17], 0\n\t"
  "v_mov_b64_e32 v[18:19], 0\n\t"
  "v_mov_b64_e32 v[20:21], 0\n\t"
  "v_mov_b64_e32 v[22:23], 0\n\t"
  "v_mov_b64_e32 v[24:25], 0\n\t"
  "v_mov_b64_e32 v[26:27], 0\n\t"
  "v_mov_b64_e32 v[28:29], 0\n\t"
  "v_mov_b64_e32 v[30:31], 0\n\t"
  "v_mov_b64_e32 v[32:33], 0\n\t"
  "v_mov_b64_e32 v[34:35], 0\n\t"
  "v_mov_b64_e32 v[36:37], 0\n\t"
  "v_mov_b64_e32 v[38:39], 0\n\t"
  "v_mov_b64_e32 v[40:41], 0\n\t"
  "v_mov_b64_e32 v[42:43], 0\n\t"
  "v_mov_b64_e32 v[44:45], 0\n\t"
  "v_mov_b64_e32 v[46:47], 0\n\t"
  "v_mov_b64_e32 v[48:49], 0\n\t"
  "v_mov_b64_e32 v[50:51], 0\n\t"
  "v_mov_b64_e32 v[52:53], 0\n\t"
  "v_mov_b64_e32 v[54:55], 0\n\t"
  "v_mov_b64_e32 v[56:57], 0\n\t"
  "v_mov_b64_e32 v[58:59], 0\n\t"
  "v_mov_b64_e32 v[60:61], 0\n\t"
  "v_mov_b64_e32 v[62:63], 0\n\t"
  "v_mov_b64_e32 v[236:237], 0\n\t"
  "v_mov_b32_e32 v136, 0x3f803f80\n\t"
  "v_mov_b32_e32 v137, 0x3f803f80\n\t"
  "v_mov_b32_e32 v138, 0x3f803f80\n\t"
  "v_mov_b32_e32 v139, 0x3f803f80\n\t"
  "s_mov_b32 s82, 0x3e0293ee\n\t"
  "s_mov_b32 s83, 0x3e0293ee\n\t"
  "s_mov_b32 s80, 0\n\t"
  "s_mov_b32 s81, 14\n\t"
  "s_mov_b32 s75, 0x4e800000\n\t"
  "s_mov_b32 s74, 32768\n\t"
  "s_mov_b32 s73, 0x1ffff\n\t"
  "s_mov_b32 s78, 0xffff0000\n\t"
  "s_waitcnt vmcnt(4)\n\t"
  "ds_read_b128 v[64:67], v243 offset:0\n\t"
  "ds_read_b128 v[68:71], v243 offset:512\n\t"
  "ds_read_b128 v[72:75], v243 offset:1024\n\t"
  "ds_read_b128 v[76:79], v243 offset:1536\n\t"
  "s_waitcnt lgkmcnt(3)\n\t"
  "v_lshlrev_b32_e32 v238, 16, v64\n\t"
  "v_and_b32_e32 v239, s78, v64\n\t"
  "v_lshlrev_b32_e32 v240, 16, v65\n\t"
  "v_and_b32_e32 v241, s78, v65\n\t"
  "v_lshlrev_b32_e32 v250, 16, v66\n\t"
  "v_and_b32_e32 v251, s78, v66\n\t"
  "v_lshlrev_b32_e32 v252, 16, v67\n\t"
  "v_and_b32_e32 v253, s78, v67\n\t"
  "v_pk_mul_f32 v[238:239], v[238:239], s[82:83]\n\t"
  "v_pk_mul_f32 v[240:241], v[240:241], s[82:83]\n\t"
  "v_pk_mul_f32 v[250:251], v[250:251], s[82:83]\n\t"
  "v_pk_mul_f32 v[252:253], v[252:253], s[82:83]\n\t"
  "v_cvt_pk_bf16_f32 v64, v238, v239\n\t"
  "v_cvt_pk_bf16_f32 v65, v240, v241\n\t"
  "v_cvt_pk_bf16_f32 v66, v250, v251\n\t"
  "v_cvt_pk_bf16_f32 v67, v252, v253\n\t"
  "s_waitcnt lgkmcnt(2)\n\t"
  "v_lshlrev_b32_e32 v238, 16, v68\n\t"
  "v_and_b32_e32 v239, s78, v68\n\t"
  "v_lshlrev_b32_e32 v240, 16, v69\n\t"
  "v_and_b32_e32 v241, s78, v69\n\t"
  "v_lshlrev_b32_e32 v250, 16, v70\n\t"
  "v_and_b32_e32 v251, s78, v70\n\t"
  "v_lshlrev_b32_e32 v252, 16, v71\n\t"
  "v_and_b32_e32 v253, s78, v71\n\t"
  "v_pk_mul_f32 v[238:239], v[238:239], s[82:83]\n\t"
  "v_pk_mul_f32 v[240:241], v[240:241], s[82:83]\n\t"
  "v_pk_mul_f32 v[250:251], v[250:251], s[82:83]\n\t"
  "v_pk_mul_f32 v[252:253], v[252:253], s[82:83]\n\t"
  "v_cvt_pk_bf16_f32 v68, v238, v239\n\t"
  "v_cvt_pk_bf16_f32 v69, v240, v241\n\t"
  "v_cvt_pk_bf16_f32 v70, v250, v251\n\t"
  "v_cvt_pk_bf16_f32 v71, v252, v253\n\t"
  "s_waitcnt lgkmcnt(1)\n\t"
  "v_lshlrev_b32_e32 v238, 16, v72\n\t"
  "v_and_b32_e32 v239, s78, v72\n\t"
  "v_lshlrev_b32_e32 v240, 16, v73\n\t"
  "v_and_b32_e32 v241, s78, v73\n\t"
  "v_lshlrev_b32_e32 v250, 16, v74\n\t"
  "v_and_b32_e32 v251, s78, v74\n\t"
  "v_lshlrev_b32_e32 v252, 16, v75\n\t"
  "v_and_b32_e32 v253, s78, v75\n\t"
  "v_pk_mul_f32 v[238:239], v[238:239], s[82:83]\n\t"
  "v_pk_mul_f32 v[240:241], v[240:241], s[82:83]\n\t"
  "v_pk_mul_f32 v[250:251], v[250:251], s[82:83]\n\t"
  "v_pk_mul_f32 v[252:253], v[252:253], s[82:83]\n\t"
  "v_cvt_pk_bf16_f32 v72, v238, v239\n\t"
  "v_cvt_pk_bf16_f32 v73, v240, v241\n\t"
  "v_cvt_pk_bf16_f32 v74, v250, v251\n\t"
  "v_cvt_pk_bf16_f32 v75, v252, v253\n\t"
  "s_waitcnt lgkmcnt(0)\n\t"
  "v_lshlrev_b32_e32 v238, 16, v76\n\t"
  "v_and_b32_e32 v239, s78, v76\n\t"
  "v_lshlrev_b32_e32 v240, 16, v77\n\t"
  "v_and_b32_e32 v241, s78, v77\n\t"
  "v_lshlrev_b32_e32 v250, 16, v78\n\t"
  "v_and_b32_e32 v251, s78, v78\n\t"
  "v_lshlrev_b32_e32 v252, 16, v79\n\t"
  "v_and_b32_e32 v253, s78, v79\n\t"
  "v_pk_mul_f32 v[238:239], v[238:239], s[82:83]\n\t"
  "v_pk_mul_f32 v[240:241], v[240:241], s[82:83]\n\t"
  "v_pk_mul_f32 v[250:251], v[250:251], s[82:83]\n\t"
  "v_pk_mul_f32 v[252:253], v[252:253], s[82:83]\n\t"
  "v_cvt_pk_bf16_f32 v76, v238, v239\n\t"
  "v_cvt_pk_bf16_f32 v77, v240, v241\n\t"
  "v_cvt_pk_bf16_f32 v78, v250, v251\n\t"
  "v_cvt_pk_bf16_f32 v79, v252, v253\n\t"
  "s_waitcnt vmcnt(0)\n\t"
  "ds_read_b128 v[80:83], v243 offset:4096\n\t"
  "ds_read_b128 v[84:87], v243 offset:4608\n\t"
  "ds_read_b128 v[88:91], v243 offset:5120\n\t"
  "ds_read_b128 v[92:95], v243 offset:5632\n\t"
  "s_waitcnt lgkmcnt(3)\n\t"
  "v_lshlrev_b32_e32 v238, 16, v80\n\t"
  "v_and_b32_e32 v239, s78, v80\n\t"
  "v_lshlrev_b32_e32 v240, 16, v81\n\t"
  "v_and_b32_e32 v241, s78, v81\n\t"
  "v_lshlrev_b32_e32 v250, 16, v82\n\t"
  "v_and_b32_e32 v251, s78, v82\n\t"
  "v_lshlrev_b32_e32 v252, 16, v83\n\t"
  "v_and_b32_e32 v253, s78, v83\n\t"
  "v_pk_mul_f32 v[238:239], v[238:239], s[82:83]\n\t"
  "v_pk_mul_f32 v[240:241], v[240:241], s[82:83]\n\t"
  "v_pk_mul_f32 v[250:251], v[250:251], s[82:83]\n\t"
  "v_pk_mul_f32 v[252:253], v[252:253], s[82:83]\n\t"
  "v_cvt_pk_bf16_f32 v80, v238, v239\n\t"
  "v_cvt_pk_bf16_f32 v81, v240, v241\n\t"
  "v_cvt_pk_bf16_f32 v82, v250, v251\n\t"
  "v_cvt_pk_bf16_f32 v83, v252, v253\n\t"
  "s_waitcnt lgkmcnt(2)\n\t"
  "v_lshlrev_b32_e32 v238, 16, v84\n\t"
  "v_and_b32_e32 v239, s78, v84\n\t"
  "v_lshlrev_b32_e32 v240, 16, v85\n\t"
  "v_and_b32_e32 v241, s78, v85\n\t"
  "v_lshlrev_b32_e32 v250, 16, v86\n\t"
  "v_and_b32_e32 v251, s78, v86\n\t"
  "v_lshlrev_b32_e32 v252, 16, v87\n\t"
  "v_and_b32_e32 v253, s78, v87\n\t"
  "v_pk_mul_f32 v[238:239], v[238:239], s[82:83]\n\t"
  "v_pk_mul_f32 v[240:241], v[240:241], s[82:83]\n\t"
  "v_pk_mul_f32 v[250:251], v[250:251], s[82:83]\n\t"
  "v_pk_mul_f32 v[252:253], v[252:253], s[82:83]\n\t"
  "v_cvt_pk_bf16_f32 v84, v238, v239\n\t"
  "v_cvt_pk_bf16_f32 v85, v240, v241\n\t"
  "v_cvt_pk_bf16_f32 v86, v250, v251\n\t"
  "v_cvt_pk_bf16_f32 v87, v252, v253\n\t"
  "s_waitcnt lgkmcnt(1)\n\t"
  "v_lshlrev_b32_e32 v238, 16, v88\n\t"
  "v_and_b32_e32 v239, s78, v88\n\t"
  "v_lshlrev_b32_e32 v240, 16, v89\n\t"
  "v_and_b32_e32 v241, s78, v89\n\t"
  "v_lshlrev_b32_e32 v250, 16, v90\n\t"
  "v_and_b32_e32 v251, s78, v90\n\t"
  "v_lshlrev_b32_e32 v252, 16, v91\n\t"
  "v_and_b32_e32 v253, s78, v91\n\t"
  "v_pk_mul_f32 v[238:239], v[238:239], s[82:83]\n\t"
  "v_pk_mul_f32 v[240:241], v[240:241], s[82:83]\n\t"
  "v_pk_mul_f32 v[250:251], v[250:251], s[82:83]\n\t"
  "v_pk_mul_f32 v[252:253], v[252:253], s[82:83]\n\t"
  "v_cvt_pk_bf16_f32 v88, v238, v239\n\t"
  "v_cvt_pk_bf16_f32 v89, v240, v241\n\t"
  "v_cvt_pk_bf16_f32 v90, v250, v251\n\t"
  "v_cvt_pk_bf16_f32 v91, v252, v253\n\t"
  "s_waitcnt lgkmcnt(0)\n\t"
  "v_lshlrev_b32_e32 v238, 16, v92\n\t"
  "v_and_b32_e32 v239, s78, v92\n\t"
  "v_lshlrev_b32_e32 v240, 16, v93\n\t"
  "v_and_b32_e32 v241, s78, v93\n\t"
  "v_lshlrev_b32_e32 v250, 16, v94\n\t"
  "v_and_b32_e32 v251, s78, v94\n\t"
  "v_lshlrev_b32_e32 v252, 16, v95\n\t"
  "v_and_b32_e32 v253, s78, v95\n\t"
  "v_pk_mul_f32 v[238:239], v[238:239], s[82:83]\n\t"
  "v_pk_mul_f32 v[240:241], v[240:241], s[82:83]\n\t"
  "v_pk_mul_f32 v[250:251], v[250:251], s[82:83]\n\t"
  "v_pk_mul_f32 v[252:253], v[252:253], s[82:83]\n\t"
  "v_cvt_pk_bf16_f32 v92, v238, v239\n\t"
  "v_cvt_pk_bf16_f32 v93, v240, v241\n\t"
  "v_cvt_pk_bf16_f32 v94, v250, v251\n\t"
  "v_cvt_pk_bf16_f32 v95, v252, v253\n\t"
  "s_barrier\n\t"
  "ds_read_b128 v[164:167], v247 offset:0\n\t"
  "ds_read_b128 v[168:171], v247 offset:512\n\t"
  "ds_read_b128 v[172:175], v247 offset:1024\n\t"
  "ds_read_b128 v[176:179], v247 offset:1536\n\t"
  "ds_read_b128 v[180:183], v247 offset:4096\n\t"
  "ds_read_b128 v[184:187], v247 offset:4608\n\t"
  "ds_read_b128 v[188:191], v247 offset:5120\n\t"
  "ds_read_b128 v[192:195], v247 offset:5632\n\t"
  "s_waitcnt lgkmcnt(7)\n\t"
  "v_mfma_f32_16x16x32_bf16 v[96:99], v[164:167], v[64:67], 0\n\t"
  "s_waitcnt lgkmcnt(6)\n\t"
  "v_mfma_f32_16x16x32_bf16 v[96:99], v[168:171], v[68:71], v[96:99]\n\t"
  "s_waitcnt lgkmcnt(5)\n\t"
  "v_mfma_f32_16x16x32_bf16 v[96:99], v[172:175], v[72:75], v[96:99]\n\t"
  "s_waitcnt lgkmcnt(4)\n\t"
  "v_mfma_f32_16x16x32_bf16 v[96:99], v[176:179], v[76:79], v[96:99]\n\t"
  "v_mfma_f32_16x16x32_bf16 v[100:103], v[164:167], v[80:83], 0\n\t"
  "v_mfma_f32_16x16x32_bf16 v[100:103], v[168:171], v[84:87], v[100:103]\n\t"
  "v_mfma_f32_16x16x32_bf16 v[100:103], v[172:175], v[88:91], v[100:103]\n\t"
  "v_mfma_f32_16x16x32_bf16 v[100:103], v[176:179], v[92:95], v[100:103]\n\t"
  "ds_read_b128 v[164:167], v247 offset:8192\n\t"
  "ds_read_b128 v[168:171], v247 offset:8704\n\t"
  "ds_read_b128 v[172:175], v247 offset:9216\n\t"
  "ds_read_b128 v[176:179], v247 offset:9728\n\t"
  "s_nop 3\n\t"
  "v_max3_f32 v238, v96, v97, v98\n\t"
  "v_max3_f32 v239, v100, v101, v102\n\t"
  "v_max_f32_e32 v238, v238, v99\n\t"
  "v_max_f32_e32 v239, v239, v103\n\t"
  "v_mov_b32_e32 v240, v238\n\t"
  "v_mov_b32_e32 v241, v239\n\t"
  "s_nop 0\n\t"
  "v_permlane16_swap_b32_e32 v238, v240\n\t"
  "v_permlane16_swap_b32_e32 v239, v241\n\t"
  "v_max_f32_e32 v238, v238, v240\n\t"
  "v_max_f32_e32 v239, v239, v241\n\t"
  "v_mov_b32_e32 v240, v238\n\t"
  "v_mov_b32_e32 v241, v239\n\t"
  "s_nop 0\n\t"
  "v_permlane32_swap_b32_e32 v238, v240\n\t"
  "v_permlane32_swap_b32_e32 v239, v241\n\t"
  "v_max_f32_e64 v128, v238, v240\n\t"
  "v_max_f32_e64 v132, v239, v241\n\t"
  "v_sub_f32_e32 v128, 0, v128\n\t"
  "v_sub_f32_e32 v132, 0, v132\n\t"
  "v_mov_b32_e32 v129, v128\n\t"
  "v_mov_b32_e32 v133, v132\n\t"
  "v_mov_b64_e32 v[130:131], v[128:129]\n\t"
  "v_mov_b64_e32 v[134:135], v[132:133]\n\t"
  "v_add_f32_e32 v96, v96, v128\n\t"
  "v_add_f32_e32 v97, v97, v128\n\t"
  "s_waitcnt lgkmcnt(4)\n\t"
  "v_mfma_f32_16x16x32_bf16 v[104:107], v[180:183], v[64:67], v[128:131]\n\t"
  "v_add_f32_e32 v98, v98, v128\n\t"
  "v_add_f32_e32 v99, v99, v128\n\t"
  "v_mfma_f32_16x16x32_bf16 v[104:107], v[184:187], v[68:71], v[104:107]\n\t"
  "v_exp_f32_e32 v228, v96\n\t"
  "v_mfma_f32_16x16x32_bf16 v[104:107], v[188:191], v[72:75], v[104:107]\n\t"
  "v_add_f32_e32 v100, v100, v132\n\t"
  "v_add_f32_e32 v101, v101, v132\n\t"
  "v_mfma_f32_16x16x32_bf16 v[104:107], v[192:195], v[76:79], v[104:107]\n\t"
  "v_exp_f32_e32 v229, v97\n\t"
  "v_mfma_f32_16x16x32_bf16 v[108:111], v[180:183], v[80:83], v[132:135]\n\t"
  "v_add_f32_e32 v102, v102, v132\n\t"
  "v_add_f32_e32 v103, v103, v132\n\t"
  "v_mfma_f32_16x16x32_bf16 v[108:111], v[184:187], v[84:87], v[108:111]\n\t"
  "v_exp_f32_e32 v230, v98\n\t"
  "v_mfma_f32_16x16x32_bf16 v[108:111], v[188:191], v[88:91], v[108:111]\n\t"
  "v_exp_f32_e32 v231, v99\n\t"
  "v_mfma_f32_16x16x32_bf16 v[108:111], v[192:195], v[92:95], v[108:111]\n\t"
  "s_waitcnt lgkmcnt(0)\n\t"
  "ds_read_b128 v[180:183], v247 offset:12288\n\t"
  "ds_read_b128 v[184:187], v247 offset:12800\n\t"
  "ds_read_b128 v[188:191], v247 offset:13312\n\t"
  "ds_read_b128 v[192:195], v247 offset:13824\n\t"
  "v_mov_b32_e32 v247, v244\n\t"
  "v_mfma_f32_16x16x32_bf16 v[112:115], v[164:167], v[64:67], v[128:131]\n\t"
  "v_exp_f32_e32 v232, v100\n\t"
  "v_cvt_pk_bf16_f32 v148, v228, v229\n\t"
  "v_mfma_f32_16x16x32_bf16 v[112:115], v[168:171], v[68:71], v[112:115]\n\t"
  "v_exp_f32_e32 v233, v101\n\t"
  "v_cvt_pk_bf16_f32 v149, v230, v231\n\t"
  "v_mfma_f32_16x16x32_bf16 v[112:115], v[172:175], v[72:75], v[112:115]\n\t"
  "v_exp_f32_e32 v234, v102\n\t"
  "v_exp_f32_e32 v235, v103\n\t"
  "v_mfma_f32_16x16x32_bf16 v[112:115], v[176:179], v[76:79], v[112:115]\n\t"
  "v_exp_f32_e32 v228, v104\n\t"
  "v_cvt_pk_bf16_f32 v152, v232, v233\n\t"
  "v_mfma_f32_16x16x32_bf16 v[116:119], v[164:167], v[80:83], v[132:135]\n\t"
  "v_exp_f32_e32 v229, v105\n\t"
  "v_cvt_pk_bf16_f32 v153, v234, v235\n\t"
  "v_mfma_f32_16x16x32_bf16 v[116:119], v[168:171], v[84:87], v[116:119]\n\t"
  "v_exp_f32_e32 v230, v106\n\t"
  "v_exp_f32_e32 v231, v107\n\t"
  "v_mfma_f32_16x16x32_bf16 v[116:119], v[172:175], v[88:91], v[116:119]\n\t"
  "v_exp_f32_e32 v232, v108\n\t"
  "v_cvt_pk_bf16_f32 v150, v228, v229\n\t"
  "v_mfma_f32_16x16x32_bf16 v[116:119], v[176:179], v[92:95], v[116:119]\n\t"
  "s_waitcnt lgkmcnt(0)\n\t"
  "s_barrier\n\t"
  "ds_read_b64_tr_b16 v[196:197], v248 offset:0\n\t"
  "ds_read_b64_tr_b16 v[198:199], v248 offset:4096\n\t"
  "ds_read_b64_tr_b16 v[200:201], v249 offset:0\n\t"
  "ds_read_b64_tr_b16 v[202:203], v249 offset:4096\n\t"
  "v_exp_f32_e32 v233, v109\n\t"
  "v_cvt_pk_bf16_f32 v151, v230, v231\n\t"
  "v_mfma_f32_16x16x32_bf16 v[120:123], v[180:183], v[64:67], v[128:131]\n\t"
  "ds_read_b64_tr_b16 v[204:205], v248 offset:512\n\t"
  "ds_read_b64_tr_b16 v[206:207], v248 offset:4608\n\t"
  "v_exp_f32_e32 v234, v110\n\t"
  "v_mfma_f32_16x16x32_bf16 v[120:123], v[184:187], v[68:71], v[120:123]\n\t"
  "ds_read_b64_tr_b16 v[208:209], v249 offset:512\n\t"
  "ds_read_b64_tr_b16 v[210:211], v249 offset:4608\n\t"
  "v_exp_f32_e32 v235, v111\n\t"
  "v_mfma_f32_16x16x32_bf16 v[140:143], v[136:139], v[148:151], 0\n\t"
  "v_mfma_f32_16x16x32_bf16 v[120:123], v[188:191], v[72:75], v[120:123]\n\t"
  "ds_read_b64_tr_b16 v[212:213], v248 offset:1024\n\t"
  "ds_read_b64_tr_b16 v[214:215], v248 offset:5120\n\t"
  "v_cvt_pk_bf16_f32 v154, v232, v233\n\t"
  "v_cvt_pk_bf16_f32 v155, v234, v235\n\t"
  "v_mfma_f32_16x16x32_bf16 v[120:123], v[192:195], v[76:79], v[120:123]\n\t"
  "ds_read_b64_tr_b16 v[216:217], v249 offset:1024\n\t"
  "ds_read_b64_tr_b16 v[218:219], v249 offset:5120\n\t"
  "v_exp_f32_e32 v228, v112\n\t"
  "v_mfma_f32_16x16x32_bf16 v[124:127], v[180:183], v[80:83], v[132:135]\n\t"
  "ds_read_b64_tr_b16 v[220:221], v248 offset:1536\n\t"
  "ds_read_b64_tr_b16 v[222:223], v248 offset:5632\n\t"
  "v_exp_f32_e32 v229, v113\n\t"
  "v_mfma_f32_16x16x32_bf16 v[144:147], v[136:139], v[152:155], 0\n\t"
  "v_mfma_f32_16x16x32_bf16 v[124:127], v[184:187], v[84:87], v[124:127]\n\t"
  "ds_read_b64_tr_b16 v[224:225], v249 offset:1536\n\t"
  "ds_read_b64_tr_b16 v[226:227], v249 offset:5632\n\t"
  "v_exp_f32_e32 v230, v114\n\t"
  "v_mfma_f32_16x16x32_bf16 v[124:127], v[188:191], v[88:91], v[124:127]\n\t"
  "ds_read_b128 v[164:167], v247 offset:0\n\t"
  "ds_read_b128 v[168:171], v247 offset:512\n\t"
  "v_exp_f32_e32 v231, v115\n\t"
  "v_mfma_f32_16x16x32_bf16 v[124:127], v[192:195], v[92:95], v[124:127]\n\t"
  "s_waitcnt lgkmcnt(10)\n\t"
  "ds_read_b128 v[172:175], v247 offset:1024\n\t"
  "ds_read_b128 v[176:179], v247 offset:1536\n\t"
  "v_max_f32_e32 v238, v140, v144\n\t"
  "v_cmp_nge_f32_e32 vcc, 0x4e800000, v238\n\t"
  "s_nop 3\n\t"
  "s_cbranch_vccz .Lmid_%=\n\t"
  "s_branch .Lrare1_%=\n\t"
  ".p2align 8\n\t"
  ".Ltop_%=:\n\t"
  "ds_read_b64_tr_b16 v[196:197], v248 offset:0\n\t"
  "ds_read_b64_tr_b16 v[198:199], v248 offset:4096\n\t"
  "ds_read_b64_tr_b16 v[200:201], v249 offset:0\n\t"
  "ds_read_b64_tr_b16 v[202:203], v249 offset:4096\n\t"
  "ds_read_b64_tr_b16 v[204:205], v248 offset:512\n\t"
  "ds_read_b64_tr_b16 v[206:207], v248 offset:4608\n\t"
  "v_cvt_pk_bf16_f32 v150, v228, v229\n\t"
  "v_cvt_pk_bf16_f32 v151, v230, v231\n\t"
  "v_add_u32_e32 v247, s80, v244\n\t"
  "v_mfma_f32_16x16x32_bf16 v[112:115], v[164:167], v[64:67], v[128:131]\n\t"
  "v_exp_f32_e32 v232, v108\n\t"
  "v_mfma_f32_16x16x32_bf16 v[112:115], v[168:171], v[68:71], v[112:115]\n\t"
  "ds_read_b64_tr_b16 v[208:209], v249 offset:512\n\t"
  "ds_read_b64_tr_b16 v[210:211], v249 offset:4608\n\t"
  "v_mfma_f32_16x16x32_bf16 v[140:143], v[136:139], v[148:151], 0\n\t"
  "v_exp_f32_e32 v233, v109\n\t"
  "v_mfma_f32_16x16x32_bf16 v[112:115], v[172:175], v[72:75], v[112:115]\n\t"
  "ds_read_b64_tr_b16 v[212:213], v248 offset:1024\n\t"
  "ds_read_b64_tr_b16 v[214:215], v248 offset:5120\n\t"
  "v_mfma_f32_16x16x32_bf16 v[112:115], v[176:179], v[76:79], v[112:115]\n\t"
  "v_exp_f32_e32 v234, v110\n\t"
  "v_mfma_f32_16x16x32_bf16 v[116:119], v[164:167], v[80:83], v[132:135]\n\t"
  "ds_read_b64_tr_b16 v[216:217], v249 offset:1024\n\t"
  "ds_read_b64_tr_b16 v[218:219], v249 offset:5120\n\t"
  "v_mfma_f32_16x16x32_bf16 v[116:119], v[168:171], v[84:87], v[116:119]\n\t"
  "v_exp_f32_e32 v235, v111\n\t"
  "v_mfma_f32_16x16x32_bf16 v[116:119], v[172:175], v[88:91], v[116:119]\n\t"
  "ds_read_b64_tr_b16 v[220:221], v248 offset:1536\n\t"
  "ds_read_b64_tr_b16 v[222:223], v248 offset:5632\n\t"
  "v_mfma_f32_16x16x32_bf16 v[116:119], v[176:179], v[92:95], v[116:119]\n\t"
  "v_cvt_pk_bf16_f32 v154, v232, v233\n\t"
  "v_cvt_pk_bf16_f32 v155, v234, v235\n\t"
  "v_mfma_f32_16x16x32_bf16 v[120:123], v[180:183], v[64:67], v[128:131]\n\t"
  "ds_read_b64_tr_b16 v[224:225], v249 offset:1536\n\t"
  "ds_read_b64_tr_b16 v[226:227], v249 offset:5632\n\t"
  "v_mfma_f32_16x16x32_bf16 v[144:147], v[136:139], v[152:155], 0\n\t"
  "v_exp_f32_e32 v228, v112\n\t"
  "v_mfma_f32_16x16x32_bf16 v[120:123], v[184:187], v[68:71], v[120:123]\n\t"
  "ds_read_b128 v[164:167], v247 offset:0\n\t"
  "ds_read_b128 v[168:171], v247 offset:512\n\t"
  "v_mfma_f32_16x16x32_bf16 v[120:123], v[188:191], v[72:75], v[120:123]\n\t"
  "v_exp_f32_e32 v229, v113\n\t"
  "v_mfma_f32_16x16x32_bf16 v[120:123], v[192:195], v[76:79], v[120:123]\n\t"
  "v_max_f32_e32 v238, v140, v144\n\t"
  "v_cmp_nge_f32_e32 vcc, 0x4e800000, v238\n\t"
  "v_mfma_f32_16x16x32_bf16 v[124:127], v[180:183], v[80:83], v[132:135]\n\t"
  "ds_read_b128 v[172:175], v247 offset:1024\n\t"
  "ds_read_b128 v[176:179], v247 offset:1536\n\t"
  "v_mfma_f32_16x16x32_bf16 v[124:127], v[184:187], v[84:87], v[124:127]\n\t"
  "s_waitcnt lgkmcnt(12)\n\t"
  "v_exp_f32_e32 v230, v114\n\t"
  "v_mfma_f32_16x16x32_bf16 v[124:127], v[188:191], v[88:91], v[124:127]\n\t"
  "s_or_b64 s[76:77], vcc, s[76:77]\n\t"
  "v_exp_f32_e32 v231, v115\n\t"
  "v_mfma_f32_16x16x32_bf16 v[124:127], v[192:195], v[92:95], v[124:127]\n\t"
  "s_cbranch_scc1 .Lslow_%=\n\t"
  ".Lret1_%=:\n\t"
  ".Lmid_%=:\n\t"
  "v_mfma_f32_16x16x32_bf16 v[0:3], v[196:199], v[148:151], v[0:3]\n\t"
  "s_add_u32 s80, s80, 0x8000\n\t"
  "v_cvt_pk_bf16_f32 v156, v228, v229\n\t"
  "v_cvt_pk_bf16_f32 v157, v230, v231\n\t"
  "v_mfma_f32_16x16x32_bf16 v[4:7], v[196:199], v[152:155], v[4:7]\n\t"
  "s_and_b32 s80, s80, 0x1ffff\n\t"
  "ds_read_b128 v[180:183], v247 offset:4096\n\t"
  "ds_read_b128 v[184:187], v247 offset:4608\n\t"
  "v_mfma_f32_16x16x32_bf16 v[8:11], v[200:203], v[148:151], v[8:11]\n\t"
  "s_sub_u32 s81, s81, 1\n\t"
  "v_exp_f32_e32 v232, v116\n\t"
  "v_mfma_f32_16x16x32_bf16 v[12:15], v[200:203], v[152:155], v[12:15]\n\t"
  "s_cselect_b64 s[76:77], -1, 0\n\t"
  "ds_read_b128 v[188:191], v247 offset:5120\n\t"
  "ds_read_b128 v[192:195], v247 offset:5632\n\t"
  "v_mfma_f32_16x16x32_bf16 v[16:19], v[204:207], v[148:151], v[16:19]\n\t"
  "v_exp_f32_e32 v233, v117\n\t"
  "v_mfma_f32_16x16x32_bf16 v[20:23], v[204:207], v[152:155], v[20:23]\n\t"
  "ds_read_b64_tr_b16 v[196:197], v248 offset:8192\n\t"
  "ds_read_b64_tr_b16 v[198:199], v248 offset:12288\n\t"
  "v_mfma_f32_16x16x32_bf16 v[24:27], v[208:211], v[148:151], v[24:27]\n\t"
  "v_exp_f32_e32 v234, v118\n\t"
  "v_mfma_f32_16x16x32_bf16 v[28:31], v[208:211], v[152:155], v[28:31]\n\t"
  "s_waitcnt lgkmcnt(10)\n\t"
  "ds_read_b64_tr_b16 v[200:201], v249 offset:8192\n\t"
  "ds_read_b64_tr_b16 v[202:203], v249 offset:12288\n\t"
  "v_mfma_f32_16x16x32_bf16 v[32:35], v[212:215], v[148:151], v[32:35]\n\t"
  "v_exp_f32_e32 v235, v119\n\t"
  "v_mfma_f32_16x16x32_bf16 v[36:39], v[212:215], v[152:155], v[36:39]\n\t"
  "v_exp_f32_e32 v228, v120\n\t"
  "v_mfma_f32_16x16x32_bf16 v[40:43], v[216:219], v[148:151], v[40:43]\n\t"
  "v_cvt_pk_bf16_f32 v160, v232, v233\n\t"
  "v_cvt_pk_bf16_f32 v161, v234, v235\n\t"
  "v_mfma_f32_16x16x32_bf16 v[44:47], v[216:219], v[152:155], v[44:47]\n\t"
  "ds_read_b64_tr_b16 v[204:205], v248 offset:8704\n\t"
  "ds_read_b64_tr_b16 v[206:207], v248 offset:12800\n\t"
  "v_mfma_f32_16x16x32_bf16 v[48:51], v[220:223], v[148:151], v[48:51]\n\t"
  "v_exp_f32_e32 v229, v121\n\t"
  "v_mfma_f32_16x16x32_bf16 v[52:55], v[220:223], v[152:155], v[52:55]\n\t"
  "v_add_f32_e32 v236, v236, v140\n\t"
  "v_add_f32_e32 v237, v237, v144\n\t"
  "v_mfma_f32_16x16x32_bf16 v[56:59], v[224:227], v[148:151], v[56:59]\n\t"
  "v_exp_f32_e32 v230, v122\n\t"
  "v_mfma_f32_16x16x32_bf16 v[60:63], v[224:227], v[152:155], v[60:63]\n\t"
  "s_waitcnt lgkmcnt(6)\n\t"
  "v_exp_f32_e32 v231, v123\n\t"
  "v_mfma_f32_16x16x32_bf16 v[96:99], v[164:167], v[64:67], v[128:131]\n\t"
  "v_cvt_pk_bf16_f32 v158, v228, v229\n\t"
  "v_cvt_pk_bf16_f32 v159, v230, v231\n\t"
  "v_mfma_f32_16x16x32_bf16 v[96:99], v[168:171], v[68:71], v[96:99]\n\t"
  "ds_read_b64_tr_b16 v[208:209], v249 offset:8704\n\t"
  "ds_read_b64_tr_b16 v[210:211], v249 offset:12800\n\t"
  "v_mfma_f32_16x16x32_bf16 v[96:99], v[172:175], v[72:75], v[96:99]\n\t"
  "v_exp_f32_e32 v232, v124\n\t"
  "v_mfma_f32_16x16x32_bf16 v[140:143], v[136:139], v[156:159], 0\n\t"
  "ds_read_b64_tr_b16 v[212:213], v248 offset:9216\n\t"
  "ds_read_b64_tr_b16 v[214:215], v248 offset:13312\n\t"
  "v_mfma_f32_16x16x32_bf16 v[96:99], v[176:179], v[76:79], v[96:99]\n\t"
  "v_exp_f32_e32 v233, v125\n\t"
  "v_mfma_f32_16x16x32_bf16 v[100:103], v[164:167], v[80:83], v[132:135]\n\t"
  "ds_read_b64_tr_b16 v[216:217], v249 offset:9216\n\t"
  "ds_read_b64_tr_b16 v[218:219], v249 offset:13312\n\t"
  "v_mfma_f32_16x16x32_bf16 v[100:103], v[168:171], v[84:87], v[100:103]\n\t"
  "v_exp_f32_e32 v234, v126\n\t"
  "v_mfma_f32_16x16x32_bf16 v[100:103], v[172:175], v[88:91], v[100:103]\n\t"
  "v_exp_f32_e32 v235, v127\n\t"
  "v_mfma_f32_16x16x32_bf16 v[100:103], v[176:179], v[92:95], v[100:103]\n\t"
  "v_cvt_pk_bf16_f32 v162, v232, v233\n\t"
  "v_cvt_pk_bf16_f32 v163, v234, v235\n\t"
  "v_mfma_f32_16x16x32_bf16 v[104:107], v[180:183], v[64:67], v[128:131]\n\t"
  "ds_read_b64_tr_b16 v[220:221], v248 offset:9728\n\t"
  "ds_read_b64_tr_b16 v[222:223], v248 offset:13824\n\t"
  "v_mfma_f32_16x16x32_bf16 v[144:147], v[136:139], v[160:163], 0\n\t"
  "v_exp_f32_e32 v228, v96\n\t"
  "v_mfma_f32_16x16x32_bf16 v[104:107], v[184:187], v[68:71], v[104:107]\n\t"
  "ds_read_b64_tr_b16 v[224:225], v249 offset:9728\n\t"
  "ds_read_b64_tr_b16 v[226:227], v249 offset:13824\n\t"
  "v_mfma_f32_16x16x32_bf16 v[104:107], v[188:191], v[72:75], v[104:107]\n\t"
  "v_exp_f32_e32 v229, v97\n\t"
  "v_mfma_f32_16x16x32_bf16 v[104:107], v[192:195], v[76:79], v[104:107]\n\t"
  "v_max_f32_e32 v238, v140, v144\n\t"
  "v_cmp_nge_f32_e32 vcc, 0x4e800000, v238\n\t"
  "v_mfma_f32_16x16x32_bf16 v[108:111], v[180:183], v[80:83], v[132:135]\n\t"
  "ds_read_b128 v[164:167], v247 offset:8192\n\t"
  "ds_read_b128 v[168:171], v247 offset:8704\n\t"
  "v_mfma_f32_16x16x32_bf16 v[108:111], v[184:187], v[84:87], v[108:111]\n\t"
  "s_waitcnt lgkmcnt(10)\n\t"
  "v_exp_f32_e32 v230, v98\n\t"
  "v_mfma_f32_16x16x32_bf16 v[108:111], v[188:191], v[88:91], v[108:111]\n\t"
  "ds_read_b128 v[172:175], v247 offset:9216\n\t"
  "ds_read_b128 v[176:179], v247 offset:9728\n\t"
  "v_mfma_f32_16x16x32_bf16 v[108:111], v[192:195], v[92:95], v[108:111]\n\t"
  "s_cbranch_vccnz .Lrare0_%=\n\t"
  ".Lret0_%=:\n\t"
  "v_mfma_f32_16x16x32_bf16 v[0:3], v[196:199], v[156:159], v[0:3]\n\t"
  "v_exp_f32_e32 v231, v99\n\t"
  "v_mfma_f32_16x16x32_bf16 v[4:7], v[196:199], v[160:163], v[4:7]\n\t"
  "ds_read_b128 v[180:183], v247 offset:12288\n\t"
  "ds_read_b128 v[184:187], v247 offset:12800\n\t"
  "v_mfma_f32_16x16x32_bf16 v[8:11], v[200:203], v[156:159], v[8:11]\n\t"
  "v_exp_f32_e32 v232, v100\n\t"
  "v_mfma_f32_16x16x32_bf16 v[12:15], v[200:203], v[160:163], v[12:15]\n\t"
  "ds_read_b128 v[188:191], v247 offset:13312\n\t"
  "ds_read_b128 v[192:195], v247 offset:13824\n\t"
  "v_mfma_f32_16x16x32_bf16 v[16:19], v[204:207], v[156:159], v[16:19]\n\t"
  "v_cvt_pk_bf16_f32 v148, v228, v229\n\t"
  "v_cvt_pk_bf16_f32 v149, v230, v231\n\t"
  "v_mfma_f32_16x16x32_bf16 v[20:23], v[204:207], v[160:163], v[20:23]\n\t"
  "v_exp_f32_e32 v233, v101\n\t"
  "v_mfma_f32_16x16x32_bf16 v[24:27], v[208:211], v[156:159], v[24:27]\n\t"
  "v_exp_f32_e32 v234, v102\n\t"
  "v_mfma_f32_16x16x32_bf16 v[28:31], v[208:211], v[160:163], v[28:31]\n\t"
  "s_waitcnt lgkmcnt(8)\n\t"
  "v_exp_f32_e32 v235, v103\n\t"
  "v_mfma_f32_16x16x32_bf16 v[32:35], v[212:215], v[156:159], v[32:35]\n\t"
  "v_exp_f32_e32 v228, v104\n\t"
  "v_mfma_f32_16x16x32_bf16 v[36:39], v[212:215], v[160:163], v[36:39]\n\t"
  "v_cvt_pk_bf16_f32 v152, v232, v233\n\t"
  "v_cvt_pk_bf16_f32 v153, v234, v235\n\t"
  "v_mfma_f32_16x16x32_bf16 v[40:43], v[216:219], v[156:159], v[40:43]\n\t"
  "v_exp_f32_e32 v229, v105\n\t"
  "v_mfma_f32_16x16x32_bf16 v[44:47], v[216:219], v[160:163], v[44:47]\n\t"
  "v_add_f32_e32 v236, v236, v140\n\t"
  "v_add_f32_e32 v237, v237, v144\n\t"
  "v_mfma_f32_16x16x32_bf16 v[48:51], v[220:223], v[156:159], v[48:51]\n\t"
  "v_exp_f32_e32 v230, v106\n\t"
  "v_mfma_f32_16x16x32_bf16 v[52:55], v[220:223], v[160:163], v[52:55]\n\t"
  "v_exp_f32_e32 v231, v107\n\t"
  "v_mfma_f32_16x16x32_bf16 v[56:59], v[224:227], v[156:159], v[56:59]\n\t"
  "v_add_u32_e32 v248, s80, v245\n\t"
  "v_add_u32_e32 v249, s80, v246\n\t"
  "v_mfma_f32_16x16x32_bf16 v[60:63], v[224:227], v[160:163], v[60:63]\n\t"
  "s_waitcnt lgkmcnt(0)\n\t"
  "s_barrier\n\t"
  "s_branch .Ltop_%=\n\t"
  ".Lslow_%=:\n\t"
  "s_cbranch_vccnz .Lrare1_%=\n\t"
  ".Lslow2_%=:\n\t"
  "s_cmp_gt_i32 s81, -1\n\t"
  "s_cbranch_scc1 .Lret1_%=\n\t"
  ".Ltail_%=:\n\t"
  "s_waitcnt lgkmcnt(0)\n\t"
  "v_mfma_f32_16x16x32_bf16 v[0:3], v[196:199], v[148:151], v[0:3]\n\t"
  "ds_read_b64_tr_b16 v[164:165], v248 offset:8192\n\t"
  "ds_read_b64_tr_b16 v[166:167], v248 offset:12288\n\t"
  "v_cvt_pk_bf16_f32 v156, v228, v229\n\t"
  "v_cvt_pk_bf16_f32 v157, v230, v231\n\t"
  "v_mfma_f32_16x16x32_bf16 v[8:11], v[200:203], v[148:151], v[8:11]\n\t"
  "ds_read_b64_tr_b16 v[168:169], v249 offset:8192\n\t"
  "ds_read_b64_tr_b16 v[170:171], v249 offset:12288\n\t"
  "v_exp_f32_e32 v228, v120\n\t"
  "v_mfma_f32_16x16x32_bf16 v[16:19], v[204:207], v[148:151], v[16:19]\n\t"
  "v_exp_f32_e32 v229, v121\n\t"
  "v_exp_f32_e32 v230, v122\n\t"
  "v_mfma_f32_16x16x32_bf16 v[24:27], v[208:211], v[148:151], v[24:27]\n\t"
  "s_waitcnt lgkmcnt(8)\n\t"
  "ds_read_b64_tr_b16 v[172:173], v248 offset:8704\n\t"
  "ds_read_b64_tr_b16 v[174:175], v248 offset:12800\n\t"
  "v_exp_f32_e32 v231, v123\n\t"
  "v_mfma_f32_16x16x32_bf16 v[32:35], v[212:215], v[148:151], v[32:35]\n\t"
  "ds_read_b64_tr_b16 v[176:177], v249 offset:8704\n\t"
  "ds_read_b64_tr_b16 v[178:179], v249 offset:12800\n\t"
  "v_exp_f32_e32 v232, v116\n\t"
  "v_mfma_f32_16x16x32_bf16 v[40:43], v[216:219], v[148:151], v[40:43]\n\t"
  "v_exp_f32_e32 v233, v117\n\t"
  "v_cvt_pk_bf16_f32 v158, v228, v229\n\t"
  "v_cvt_pk_bf16_f32 v159, v230, v231\n\t"
  "v_mfma_f32_16x16x32_bf16 v[48:51], v[220:223], v[148:151], v[48:51]\n\t"
  "ds_read_b64_tr_b16 v[180:181], v248 offset:9216\n\t"
  "ds_read_b64_tr_b16 v[182:183], v248 offset:13312\n\t"
  "v_exp_f32_e32 v234, v118\n\t"
  "v_mfma_f32_16x16x32_bf16 v[56:59], v[224:227], v[148:151], v[56:59]\n\t"
  "ds_read_b64_tr_b16 v[184:185], v249 offset:9216\n\t"
  "ds_read_b64_tr_b16 v[186:187], v249 offset:13312\n\t"
  "v_exp_f32_e32 v235, v119\n\t"
  "v_mfma_f32_16x16x32_bf16 v[4:7], v[196:199], v[152:155], v[4:7]\n\t"
  "v_add_f32_e32 v236, v236, v140\n\t"
  "v_add_f32_e32 v237, v237, v144\n\t"
  "v_cvt_pk_bf16_f32 v160, v232, v233\n\t"
  "v_cvt_pk_bf16_f32 v161, v234, v235\n\t"
  "v_mfma_f32_16x16x32_bf16 v[12:15], v[200:203], v[152:155], v[12:15]\n\t"
  "ds_read_b64_tr_b16 v[188:189], v248 offset:9728\n\t"
  "ds_read_b64_tr_b16 v[190:191], v248 offset:13824\n\t"
  "v_exp_f32_e32 v232, v124\n\t"
  "v_mfma_f32_16x16x32_bf16 v[140:143], v[136:139], v[156:159], 0\n\t"
  "v_mfma_f32_16x16x32_bf16 v[20:23], v[204:207], v[152:155], v[20:23]\n\t"
  "ds_read_b64_tr_b16 v[192:193], v249 offset:9728\n\t"
  "ds_read_b64_tr_b16 v[194:195], v249 offset:13824\n\t"
  "v_exp_f32_e32 v233, v125\n\t"
  "v_mfma_f32_16x16x32_bf16 v[28:31], v[208:211], v[152:155], v[28:31]\n\t"
  "v_exp_f32_e32 v234, v126\n\t"
  "v_mbcnt_lo_u32_b32 v250, -1, 0\n\t"
  "v_mfma_f32_16x16x32_bf16 v[36:39], v[212:215], v[152:155], v[36:39]\n\t"
  "v_exp_f32_e32 v235, v127\n\t"
  "v_mbcnt_hi_u32_b32 v250, -1, v250\n\t"
  "v_mfma_f32_16x16x32_bf16 v[44:47], v[216:219], v[152:155], v[44:47]\n\t"
  "v_cvt_pk_bf16_f32 v162, v232, v233\n\t"
  "v_cvt_pk_bf16_f32 v163, v234, v235\n\t"
  "v_mfma_f32_16x16x32_bf16 v[52:55], v[220:223], v[152:155], v[52:55]\n\t"
  "v_lshrrev_b32_e32 v251, 4, v250\n\t"
  "v_and_b32_e32 v252, 15, v250\n\t"
  "v_mfma_f32_16x16x32_bf16 v[144:147], v[136:139], v[160:163], 0\n\t"
  "v_mfma_f32_16x16x32_bf16 v[60:63], v[224:227], v[152:155], v[60:63]\n\t"
  "v_mul_u32_u24_e32 v247, 0x108, v252\n\t"
  "v_mul_u32_u24_e32 v244, 0x108, v251\n\t"
  "v_lshl_add_u32 v247, v251, 3, v247\n\t"
  "v_lshl_add_u32 v244, v252, 4, v244\n\t"
  "s_mul_i32 s49, %2, 8448\n\t"
  "s_add_u32 s49, s49, %3\n\t"
  "s_add_u32 s49, s49, 65536\n\t"
  "v_max_f32_e32 v238, v140, v144\n\t"
  "v_cmp_nge_f32_e32 vcc, 0x4e800000, v238\n\t"
  "v_add_u32_e32 v247, s49, v247\n\t"
  "v_add_u32_e32 v244, s49, v244\n\t"
  "v_lshlrev_b32_e32 v245, 4, v250\n\t"
  "v_add_u32_e32 v246, 0x1000, v245\n\t"
  "s_waitcnt lgkmcnt(0)\n\t"
  "s_cbranch_vccnz .Lrare2_%=\n\t"
  ".Lret2_%=:\n\t"
  "v_mfma_f32_16x16x32_bf16 v[0:3], v[164:167], v[156:159], v[0:3]\n\t"
  "v_add_f32_e32 v236, v236, v140\n\t"
  "v_add_f32_e32 v237, v237, v144\n\t"
  "v_mfma_f32_16x16x32_bf16 v[8:11], v[168:171], v[156:159], v[8:11]\n\t"
  "v_rcp_f32_e32 v238, v236\n\t"
  "v_mfma_f32_16x16x32_bf16 v[16:19], v[172:175], v[156:159], v[16:19]\n\t"
  "v_rcp_f32_e32 v240, v237\n\t"
  "v_mfma_f32_16x16x32_bf16 v[24:27], v[176:179], v[156:159], v[24:27]\n\t"
  "v_mul_f32_e32 v0, v0, v238\n\t"
  "v_mul_f32_e32 v1, v1, v238\n\t"
  "v_mul_f32_e32 v2, v2, v238\n\t"
  "v_mul_f32_e32 v3, v3, v238\n\t"
  "v_mfma_f32_16x16x32_bf16 v[32:35], v[180:183], v[156:159], v[32:35]\n\t"
  "v_cvt_pk_bf16_f32 v96, v0, v1\n\t"
  "v_cvt_pk_bf16_f32 v97, v2, v3\n\t"
  "v_mul_f32_e32 v8, v8, v238\n\t"
  "v_mul_f32_e32 v9, v9, v238\n\t"
  "v_mfma_f32_16x16x32_bf16 v[40:43], v[184:187], v[156:159], v[40:43]\n\t"
  "v_mul_f32_e32 v10, v10, v238\n\t"
  "v_mul_f32_e32 v11, v11, v238\n\t"
  "v_cvt_pk_bf16_f32 v100, v8, v9\n\t"
  "v_cvt_pk_bf16_f32 v101, v10, v11\n\t"
  "v_mfma_f32_16x16x32_bf16 v[48:51], v[188:191], v[156:159], v[48:51]\n\t"
  "v_mul_f32_e32 v16, v16, v238\n\t"
  "v_mul_f32_e32 v17, v17, v238\n\t"
  "v_mul_f32_e32 v18, v18, v238\n\t"
  "v_mfma_f32_16x16x32_bf16 v[56:59], v[192:195], v[156:159], v[56:59]\n\t"
  "v_mul_f32_e32 v19, v19, v238\n\t"
  "v_cvt_pk_bf16_f32 v104, v16, v17\n\t"
  "v_cvt_pk_bf16_f32 v105, v18, v19\n\t"
  "ds_write_b64 v247, v[96:97] offset:0\n\t"
  "ds_write_b64 v247, v[100:101] offset:32\n\t"
  "ds_write_b64 v247, v[104:105] offset:64\n\t"
  "v_pk_mul_f32 v[24:25], v[24:25], v[238:239] op_sel_hi:[1,0]\n\t"
  "v_pk_mul_f32 v[26:27], v[26:27], v[238:239] op_sel_hi:[1,0]\n\t"
  "v_cvt_pk_bf16_f32 v108, v24, v25\n\t"
  "v_cvt_pk_bf16_f32 v109, v26, v27\n\t"
  "v_pk_mul_f32 v[32:33], v[32:33], v[238:239] op_sel_hi:[1,0]\n\t"
  "v_pk_mul_f32 v[34:35], v[34:35], v[238:239] op_sel_hi:[1,0]\n\t"
  "v_cvt_pk_bf16_f32 v112, v32, v33\n\t"
  "v_cvt_pk_bf16_f32 v113, v34, v35\n\t"
  "v_pk_mul_f32 v[40:41], v[40:41], v[238:239] op_sel_hi:[1,0]\n\t"
  "v_pk_mul_f32 v[42:43], v[42:43], v[238:239] op_sel_hi:[1,0]\n\t"
  "v_cvt_pk_bf16_f32 v116, v40, v41\n\t"
  "v_cvt_pk_bf16_f32 v117, v42, v43\n\t"
  "v_pk_mul_f32 v[48:49], v[48:49], v[238:239] op_sel_hi:[1,0]\n\t"
  "v_pk_mul_f32 v[50:51], v[50:51], v[238:239] op_sel_hi:[1,0]\n\t"
  "v_cvt_pk_bf16_f32 v120, v48, v49\n\t"
  "v_cvt_pk_bf16_f32 v121, v50, v51\n\t"
  "v_pk_mul_f32 v[56:57], v[56:57], v[238:239] op_sel_hi:[1,0]\n\t"
  "v_pk_mul_f32 v[58:59], v[58:59], v[238:239] op_sel_hi:[1,0]\n\t"
  "v_cvt_pk_bf16_f32 v124, v56, v57\n\t"
  "v_cvt_pk_bf16_f32 v125, v58, v59\n\t"
  "ds_write_b64 v247, v[108:109] offset:96\n\t"
  "ds_write_b64 v247, v[112:113] offset:128\n\t"
  "ds_write_b64 v247, v[116:117] offset:160\n\t"
  "ds_write_b64 v247, v[120:121] offset:192\n\t"
  "ds_write_b64 v247, v[124:125] offset:224\n\t"
  "ds_read_b64 v[196:197], v244 offset:0\n\t"
  "ds_read_b64 v[198:199], v244 offset:8\n\t"
  "ds_read_b64 v[200:201], v244 offset:1056\n\t"
  "ds_read_b64 v[202:203], v244 offset:1064\n\t"
  "ds_read_b64 v[204:205], v244 offset:2112\n\t"
  "ds_read_b64 v[206:207], v244 offset:2120\n\t"
  "ds_read_b64 v[208:209], v244 offset:3168\n\t"
  "ds_read_b64 v[210:211], v244 offset:3176\n\t"
  "v_mfma_f32_16x16x32_bf16 v[4:7], v[164:167], v[160:163], v[4:7]\n\t"
  "s_waitcnt lgkmcnt(6)\n\t"
  "global_store_dwordx4 v245, v[196:199], s[58:59] offset:0 nt\n\t"
  "v_mfma_f32_16x16x32_bf16 v[12:15], v[168:171], v[160:163], v[12:15]\n\t"
  "s_waitcnt lgkmcnt(4)\n\t"
  "global_store_dwordx4 v245, v[200:203], s[58:59] offset:1024 nt\n\t"
  "v_mfma_f32_16x16x32_bf16 v[20:23], v[172:175], v[160:163], v[20:23]\n\t"
  "s_waitcnt lgkmcnt(2)\n\t"
  "global_store_dwordx4 v245, v[204:207], s[58:59] offset:2048 nt\n\t"
  "v_mfma_f32_16x16x32_bf16 v[28:31], v[176:179], v[160:163], v[28:31]\n\t"
  "s_waitcnt lgkmcnt(0)\n\t"
  "global_store_dwordx4 v245, v[208:211], s[58:59] offset:3072 nt\n\t"
  "v_mul_f32_e32 v4, v4, v240\n\t"
  "v_mul_f32_e32 v5, v5, v240\n\t"
  "v_mul_f32_e32 v6, v6, v240\n\t"
  "v_mul_f32_e32 v7, v7, v240\n\t"
  "v_mfma_f32_16x16x32_bf16 v[36:39], v[180:183], v[160:163], v[36:39]\n\t"
  "v_cvt_pk_bf16_f32 v98, v4, v5\n\t"
  "v_cvt_pk_bf16_f32 v99, v6, v7\n\t"
  "v_mul_f32_e32 v12, v12, v240\n\t"
  "v_mul_f32_e32 v13, v13, v240\n\t"
  "v_mfma_f32_16x16x32_bf16 v[44:47], v[184:187], v[160:163], v[44:47]\n\t"
  "v_mul_f32_e32 v14, v14, v240\n\t"
  "v_mul_f32_e32 v15, v15, v240\n\t"
  "v_cvt_pk_bf16_f32 v102, v12, v13\n\t"
  "v_cvt_pk_bf16_f32 v103, v14, v15\n\t"
  "v_mfma_f32_16x16x32_bf16 v[52:55], v[188:191], v[160:163], v[52:55]\n\t"
  "v_mul_f32_e32 v20, v20, v240\n\t"
  "v_mul_f32_e32 v21, v21, v240\n\t"
  "v_mul_f32_e32 v22, v22, v240\n\t"
  "v_mfma_f32_16x16x32_bf16 v[60:63], v[192:195], v[160:163], v[60:63]\n\t"
  "v_mul_f32_e32 v23, v23, v240\n\t"
  "v_cvt_pk_bf16_f32 v106, v20, v21\n\t"
  "v_cvt_pk_bf16_f32 v107, v22, v23\n\t"
  "ds_write_b64 v247, v[98:99] offset:4224\n\t"
  "ds_write_b64 v247, v[102:103] offset:4256\n\t"
  "ds_write_b64 v247, v[106:107] offset:4288\n\t"
  "v_pk_mul_f32 v[28:29], v[28:29], v[240:241] op_sel_hi:[1,0]\n\t"
  "v_pk_mul_f32 v[30:31], v[30:31], v[240:241] op_sel_hi:[1,0]\n\t"
  "v_cvt_pk_bf16_f32 v110, v28, v29\n\t"
  "v_cvt_pk_bf16_f32 v111, v30, v31\n\t"
  "v_pk_mul_f32 v[36:37], v[36:37], v[240:241] op_sel_hi:[1,0]\n\t"
  "v_pk_mul_f32 v[38:39], v[38:39], v[240:241] op_sel_hi:[1,0]\n\t"
  "v_cvt_pk_bf16_f32 v114, v36, v37\n\t"
  "v_cvt_pk_bf16_f32 v115, v38, v39\n\t"
  "v_pk_mul_f32 v[44:45], v[44:45], v[240:241] op_sel_hi:[1,0]\n\t"
  "v_pk_mul_f32 v[46:47], v[46:47], v[240:241] op_sel_hi:[1,0]\n\t"
  "v_cvt_pk_bf16_f32 v118, v44, v45\n\t"
  "v_cvt_pk_bf16_f32 v119, v46, v47\n\t"
  "v_pk_mul_f32 v[52:53], v[52:53], v[240:241] op_sel_hi:[1,0]\n\t"
  "v_pk_mul_f32 v[54:55], v[54:55], v[240:241] op_sel_hi:[1,0]\n\t"
  "v_cvt_pk_bf16_f32 v122, v52, v53\n\t"
  "v_cvt_pk_bf16_f32 v123, v54, v55\n\t"
  "v_pk_mul_f32 v[60:61], v[60:61], v[240:241] op_sel_hi:[1,0]\n\t"
  "v_pk_mul_f32 v[62:63], v[62:63], v[240:241] op_sel_hi:[1,0]\n\t"
  "v_cvt_pk_bf16_f32 v126, v60, v61\n\t"
  "v_cvt_pk_bf16_f32 v127, v62, v63\n\t"
  "ds_write_b64 v247, v[110:111] offset:4320\n\t"
  "ds_write_b64 v247, v[114:115] offset:4352\n\t"
  "ds_write_b64 v247, v[118:119] offset:4384\n\t"
  "ds_write_b64 v247, v[122:123] offset:4416\n\t"
  "ds_write_b64 v247, v[126:127] offset:4448\n\t"
  "ds_read_b64 v[212:213], v244 offset:4224\n\t"
  "ds_read_b64 v[214:215], v244 offset:4232\n\t"
  "ds_read_b64 v[216:217], v244 offset:5280\n\t"
  "ds_read_b64 v[218:219], v244 offset:5288\n\t"
  "ds_read_b64 v[220:221], v244 offset:6336\n\t"
  "ds_read_b64 v[222:223], v244 offset:6344\n\t"
  "ds_read_b64 v[224:225], v244 offset:7392\n\t"
  "ds_read_b64 v[226:227], v244 offset:7400\n\t"
  "s_waitcnt lgkmcnt(6)\n\t"
  "global_store_dwordx4 v246, v[212:215], s[58:59] offset:0 nt\n\t"
  "s_waitcnt lgkmcnt(4)\n\t"
  "global_store_dwordx4 v246, v[216:219], s[58:59] offset:1024 nt\n\t"
  "s_waitcnt lgkmcnt(2)\n\t"
  "global_store_dwordx4 v246, v[220:223], s[58:59] offset:2048 nt\n\t"
  "s_waitcnt lgkmcnt(0)\n\t"
  "global_store_dwordx4 v246, v[224:227], s[58:59] offset:3072 nt\n\t"
  "s_endpgm\n\t"
  ".Lhelper_%=:\n\t"
  "s_mov_b32 s92, 0\n\t"
  "s_mov_b32 s60, 98304\n\t"
  "s_mov_b32 s70, 0x3e0293ee\n\t"
  "s_mov_b32 s65, 12\n\t"
  "s_lshl_b32 s50, s50, 13\n\t"
  "s_sub_u32 s84, s50, s52\n\t"
  "s_add_u32 s85, s84, 0x1000\n\t"
  "s_add_u32 s50, s50, %3\n\t"
  "s_mov_b32 s83, s50\n\t"
  "v_lshrrev_b32_e32 v251, 4, v250\n\t"
  "v_and_b32_e32 v252, 15, v250\n\t"
  "v_lshrrev_b32_e32 v183, 3, v252\n\t"
  "v_lshlrev_b32_e32 v184, 11, v183\n\t"
  "v_lshlrev_b32_e32 v183, 1, v183\n\t"
  "v_xor_b32_e32 v183, v183, v251\n\t"
  "v_lshl_add_u32 v184, v183, 4, v184\n\t"
  "v_and_b32_e32 v183, 7, v252\n\t"
  "v_lshl_add_u32 v184, v183, 6, v184\n\t"
  "v_add_u32_e32 v188, s50, v184\n\t"
  "v_add_u32_e32 v176, s80, v184\n\t"
  "v_add_u32_e32 v176, 0x1c000, v176\n\t"
  "v_lshrrev_b32_e32 v183, 1, v251\n\t"
  "v_lshlrev_b32_e32 v184, 11, v183\n\t"
  "v_and_b32_e32 v183, 1, v251\n\t"
  "v_mul_u32_u24_e32 v183, 0x120, v183\n\t"
  "v_add_u32_e32 v184, v184, v183\n\t"
  "v_lshrrev_b32_e32 v183, 2, v252\n\t"
  "v_lshl_add_u32 v184, v183, 6, v184\n\t"
  "v_and_b32_e32 v183, 3, v252\n\t"
  "v_lshl_add_u32 v184, v183, 3, v184\n\t"
  "v_add_u32_e32 v184, 0x4000, v184\n\t"
  "v_add_u32_e32 v189, s50, v184\n\t"
  "v_xor_b32_e32 v179, 32, v189\n\t"
  "v_mov_b64_e32 v[80:81], 0\n\t"
  "v_mov_b64_e32 v[82:83], 0\n\t"
  "v_mov_b64_e32 v[84:85], 0\n\t"
  "v_mov_b64_e32 v[86:87], 0\n\t"
  "v_mov_b64_e32 v[88:89], 0\n\t"
  "v_mov_b64_e32 v[90:91], 0\n\t"
  "v_mov_b64_e32 v[92:93], 0\n\t"
  "v_mov_b64_e32 v[94:95], 0\n\t"
  "v_mov_b64_e32 v[96:97], 0\n\t"
  "v_mov_b64_e32 v[98:99], 0\n\t"
  "v_mov_b64_e32 v[100:101], 0\n\t"
  "v_mov_b64_e32 v[102:103], 0\n\t"
  "v_mov_b64_e32 v[104:105], 0\n\t"
  "v_mov_b64_e32 v[106:107], 0\n\t"
  "v_mov_b64_e32 v[108:109], 0\n\t"
  "v_mov_b64_e32 v[110:111], 0\n\t"
  "v_mov_b32_e32 v124, 0x3f803f80\n\t"
  "v_mov_b32_e32 v125, 0x3f803f80\n\t"
  "v_mov_b32_e32 v126, 0x3f803f80\n\t"
  "v_mov_b32_e32 v127, 0x3f803f80\n\t"
  "v_mov_b64_e32 v[120:121], 0\n\t"
  "v_mov_b64_e32 v[122:123], 0\n\t"
  "v_mov_b32_e32 v181, 0\n\t"
  "s_mov_b32 s69, 0xffff0000\n\t"
  "s_mov_b32 s71, 0x3e0293ee\n\t"
  "s_waitcnt vmcnt(4)\n\t"
  "s_add_u32 s68, s61, s80\n\t"
  "s_add_u32 m0, s68, 0x4000\n\t"
  "ds_read_b128 v[64:67], v176 offset:0\n\t"
  "ds_read_b128 v[68:71], v176 offset:512\n\t"
  "ds_read_b128 v[72:75], v176 offset:1024\n\t"
  "ds_read_b128 v[76:79], v176 offset:1536\n\t"
  "buffer_load_dwordx4 v186, s[76:79], s63 offen offset:0 nt lds\n\t"
  "buffer_load_dwordx4 v187, s[76:79], s63 offen offset:1024 nt lds\n\t"
  "s_waitcnt lgkmcnt(3)\n\t"
  "v_lshlrev_b32_e32 v168, 16, v64\n\t"
  "v_and_b32_e32 v169, s69, v64\n\t"
  "v_lshlrev_b32_e32 v170, 16, v65\n\t"
  "v_and_b32_e32 v171, s69, v65\n\t"
  "v_lshlrev_b32_e32 v172, 16, v66\n\t"
  "v_and_b32_e32 v173, s69, v66\n\t"
  "v_lshlrev_b32_e32 v174, 16, v67\n\t"
  "v_and_b32_e32 v175, s69, v67\n\t"
  "v_pk_mul_f32 v[168:169], v[168:169], s[70:71]\n\t"
  "v_pk_mul_f32 v[170:171], v[170:171], s[70:71]\n\t"
  "v_pk_mul_f32 v[172:173], v[172:173], s[70:71]\n\t"
  "v_pk_mul_f32 v[174:175], v[174:175], s[70:71]\n\t"
  "v_cvt_pk_bf16_f32 v64, v168, v169\n\t"
  "v_cvt_pk_bf16_f32 v65, v170, v171\n\t"
  "v_cvt_pk_bf16_f32 v66, v172, v173\n\t"
  "v_cvt_pk_bf16_f32 v67, v174, v175\n\t"
  "buffer_load_dwordx4 v186, s[76:79], s63 offen offset:2048 nt lds\n\t"
  "buffer_load_dwordx4 v187, s[76:79], s63 offen offset:3072 nt lds\n\t"
  "s_add_u32 m0, s61, s80\n\t"
  "s_waitcnt lgkmcnt(2)\n\t"
  "v_lshlrev_b32_e32 v168, 16, v68\n\t"
  "v_and_b32_e32 v169, s69, v68\n\t"
  "v_lshlrev_b32_e32 v170, 16, v69\n\t"
  "v_and_b32_e32 v171, s69, v69\n\t"
  "v_lshlrev_b32_e32 v172, 16, v70\n\t"
  "v_and_b32_e32 v173, s69, v70\n\t"
  "v_lshlrev_b32_e32 v174, 16, v71\n\t"
  "v_and_b32_e32 v175, s69, v71\n\t"
  "v_pk_mul_f32 v[168:169], v[168:169], s[70:71]\n\t"
  "v_pk_mul_f32 v[170:171], v[170:171], s[70:71]\n\t"
  "v_pk_mul_f32 v[172:173], v[172:173], s[70:71]\n\t"
  "v_pk_mul_f32 v[174:175], v[174:175], s[70:71]\n\t"
  "v_cvt_pk_bf16_f32 v68, v168, v169\n\t"
  "v_cvt_pk_bf16_f32 v69, v170, v171\n\t"
  "v_cvt_pk_bf16_f32 v70, v172, v173\n\t"
  "v_cvt_pk_bf16_f32 v71, v174, v175\n\t"
  "buffer_load_dwordx4 v253, s[72:75], s62 offen offset:0 nt lds\n\t"
  "buffer_load_dwordx4 v254, s[72:75], s62 offen offset:1024 nt lds\n\t"
  "s_waitcnt lgkmcnt(1)\n\t"
  "v_lshlrev_b32_e32 v168, 16, v72\n\t"
  "v_and_b32_e32 v169, s69, v72\n\t"
  "v_lshlrev_b32_e32 v170, 16, v73\n\t"
  "v_and_b32_e32 v171, s69, v73\n\t"
  "v_lshlrev_b32_e32 v172, 16, v74\n\t"
  "v_and_b32_e32 v173, s69, v74\n\t"
  "v_lshlrev_b32_e32 v174, 16, v75\n\t"
  "v_and_b32_e32 v175, s69, v75\n\t"
  "v_pk_mul_f32 v[168:169], v[168:169], s[70:71]\n\t"
  "v_pk_mul_f32 v[170:171], v[170:171], s[70:71]\n\t"
  "v_pk_mul_f32 v[172:173], v[172:173], s[70:71]\n\t"
  "v_pk_mul_f32 v[174:175], v[174:175], s[70:71]\n\t"
  "v_cvt_pk_bf16_f32 v72, v168, v169\n\t"
  "v_cvt_pk_bf16_f32 v73, v170, v171\n\t"
  "v_cvt_pk_bf16_f32 v74, v172, v173\n\t"
  "v_cvt_pk_bf16_f32 v75, v174, v175\n\t"
  "buffer_load_dwordx4 v255, s[72:75], s62 offen offset:2048 nt lds\n\t"
  "buffer_load_dwordx4 v243, s[72:75], s62 offen offset:3072 nt lds\n\t"
  "s_mov_b32 s63, s62\n\t"
  "s_add_u32 s62, s62, 0x4000\n\t"
  "s_and_b32 s62, s62, 0x3ffff\n\t"
  "s_waitcnt lgkmcnt(0)\n\t"
  "v_lshlrev_b32_e32 v168, 16, v76\n\t"
  "v_and_b32_e32 v169, s69, v76\n\t"
  "v_lshlrev_b32_e32 v170, 16, v77\n\t"
  "v_and_b32_e32 v171, s69, v77\n\t"
  "v_lshlrev_b32_e32 v172, 16, v78\n\t"
  "v_and_b32_e32 v173, s69, v78\n\t"
  "v_lshlrev_b32_e32 v174, 16, v79\n\t"
  "v_and_b32_e32 v175, s69, v79\n\t"
  "v_pk_mul_f32 v[168:169], v[168:169], s[70:71]\n\t"
  "v_pk_mul_f32 v[170:171], v[170:171], s[70:71]\n\t"
  "v_pk_mul_f32 v[172:173], v[172:173], s[70:71]\n\t"
  "v_pk_mul_f32 v[174:175], v[174:175], s[70:71]\n\t"
  "v_cvt_pk_bf16_f32 v76, v168, v169\n\t"
  "v_cvt_pk_bf16_f32 v77, v170, v171\n\t"
  "v_cvt_pk_bf16_f32 v78, v172, v173\n\t"
  "v_cvt_pk_bf16_f32 v79, v174, v175\n\t"
  "s_mov_b32 s61, 32768\n\t"
  "s_waitcnt vmcnt(8)\n\t"
  "s_barrier\n\t"
  "s_setprio 0\n\t"
  "s_add_u32 s68, s61, s80\n\t"
  "s_add_u32 m0, s68, 0x4000\n\t"
  "s_nop 0\n\t"
  "buffer_load_dwordx4 v186, s[76:79], s63 offen offset:0 nt lds\n\t"
  "buffer_load_dwordx4 v187, s[76:79], s63 offen offset:1024 nt lds\n\t"
  "buffer_load_dwordx4 v186, s[76:79], s63 offen offset:2048 nt lds\n\t"
  "buffer_load_dwordx4 v187, s[76:79], s63 offen offset:3072 nt lds\n\t"
  "s_add_u32 m0, s61, s80\n\t"
  "s_nop 0\n\t"
  "buffer_load_dwordx4 v253, s[72:75], s62 offen offset:0 nt lds\n\t"
  "buffer_load_dwordx4 v254, s[72:75], s62 offen offset:1024 nt lds\n\t"
  "buffer_load_dwordx4 v255, s[72:75], s62 offen offset:2048 nt lds\n\t"
  "buffer_load_dwordx4 v243, s[72:75], s62 offen offset:3072 nt lds\n\t"
  "s_mov_b32 s63, s62\n\t"
  "s_add_u32 s62, s62, 0x4000\n\t"
  "s_and_b32 s62, s62, 0x3ffff\n\t"
  "s_mov_b32 s61, 65536\n\t"
  "v_add_u32_e32 v176, s60, v188\n\t"
  "ds_read_b128 v[136:139], v176 offset:0\n\t"
  "ds_read_b128 v[140:143], v176 offset:4096\n\t"
  "ds_read_b128 v[144:147], v176 offset:512\n\t"
  "ds_read_b128 v[148:151], v176 offset:4608\n\t"
  "ds_read_b128 v[152:155], v176 offset:1024\n\t"
  "ds_read_b128 v[156:159], v176 offset:5120\n\t"
  "ds_read_b128 v[160:163], v176 offset:1536\n\t"
  "ds_read_b128 v[164:167], v176 offset:5632\n\t"
  "v_mov_b32_e32 v184, %3\n\t"
  "v_mov_b32_e32 v185, 0\n\t"
  "v_add_u32_e32 v194, 0x20000, v184\n\t"
  "ds_write_b32 v194, v185\n\t"
  "s_waitcnt vmcnt(8)\n\t"
  "s_setprio 3\n\t"
  "s_waitcnt vmcnt(8) lgkmcnt(0)\n\t"
  "s_barrier\n\t"
  "v_mfma_f32_16x16x32_bf16 v[112:115], v[136:139], v[64:67], v[120:123]\n\t"
  "v_mfma_f32_16x16x32_bf16 v[112:115], v[144:147], v[68:71], v[112:115]\n\t"
  "v_mfma_f32_16x16x32_bf16 v[112:115], v[152:155], v[72:75], v[112:115]\n\t"
  "v_mfma_f32_16x16x32_bf16 v[112:115], v[160:163], v[76:79], v[112:115]\n\t"
  "v_mfma_f32_16x16x32_bf16 v[116:119], v[140:143], v[64:67], v[120:123]\n\t"
  "v_mfma_f32_16x16x32_bf16 v[116:119], v[148:151], v[68:71], v[116:119]\n\t"
  "v_mfma_f32_16x16x32_bf16 v[116:119], v[156:159], v[72:75], v[116:119]\n\t"
  "v_mfma_f32_16x16x32_bf16 v[116:119], v[164:167], v[76:79], v[116:119]\n\t"
  "s_add_u32 s68, s61, s80\n\t"
  "s_add_u32 m0, s68, 0x4000\n\t"
  "s_nop 0\n\t"
  "buffer_load_dwordx4 v186, s[76:79], s63 offen offset:0 nt lds\n\t"
  "buffer_load_dwordx4 v187, s[76:79], s63 offen offset:1024 nt lds\n\t"
  "buffer_load_dwordx4 v186, s[76:79], s63 offen offset:2048 nt lds\n\t"
  "buffer_load_dwordx4 v187, s[76:79], s63 offen offset:3072 nt lds\n\t"
  "s_add_u32 m0, s61, s80\n\t"
  "s_nop 0\n\t"
  "v_max3_f32 v182, v112, v113, v114\n\t"
  "v_max3_f32 v182, v182, v115, v116\n\t"
  "v_max3_f32 v182, v182, v117, v118\n\t"
  "v_max_f32_e32 v182, v182, v119\n\t"
  "v_mov_b32_e32 v183, v182\n\t"
  "s_nop 1\n\t"
  "v_permlane16_swap_b32_e32 v182, v183\n\t"
  "v_max_f32_e32 v182, v182, v183\n\t"
  "v_mov_b32_e32 v183, v182\n\t"
  "s_nop 1\n\t"
  "v_permlane32_swap_b32_e32 v182, v183\n\t"
  "v_max_f32_e32 v182, v182, v183\n\t"
  "v_sub_f32_e32 v112, v112, v182\n\t"
  "v_sub_f32_e32 v113, v113, v182\n\t"
  "v_sub_f32_e32 v114, v114, v182\n\t"
  "v_sub_f32_e32 v115, v115, v182\n\t"
  "v_sub_f32_e32 v116, v116, v182\n\t"
  "v_sub_f32_e32 v117, v117, v182\n\t"
  "v_sub_f32_e32 v118, v118, v182\n\t"
  "v_sub_f32_e32 v119, v119, v182\n\t"
  "v_sub_f32_e32 v120, v120, v182\n\t"
  "v_sub_f32_e32 v121, v121, v182\n\t"
  "v_sub_f32_e32 v122, v122, v182\n\t"
  "v_sub_f32_e32 v123, v123, v182\n\t"
  "buffer_load_dwordx4 v253, s[72:75], s62 offen offset:0 nt lds\n\t"
  "buffer_load_dwordx4 v254, s[72:75], s62 offen offset:1024 nt lds\n\t"
  "buffer_load_dwordx4 v255, s[72:75], s62 offen offset:2048 nt lds\n\t"
  "buffer_load_dwordx4 v243, s[72:75], s62 offen offset:3072 nt lds\n\t"
  "s_mov_b32 s63, s62\n\t"
  "s_add_u32 s62, s62, 0x4000\n\t"
  "s_and_b32 s62, s62, 0x3ffff\n\t"
  "s_cmp_eq_u32 s81, 0\n\t"
  "s_cbranch_scc1 .Lh2b_%=\n\t"
  "s_branch .Lh1b_%=\n\t"
  ".Lh1pre_%=:\n\t"
  "s_setprio 3\n\t"
  "s_waitcnt vmcnt(8) lgkmcnt(0)\n\t"
  "s_barrier\n\t"
  ".Lh1_%=:\n\t"
  "v_mfma_f32_16x16x32_bf16 v[80:83], v[136:139], v[132:135], v[80:83]\n\t"
  "v_mfma_f32_16x16x32_bf16 v[84:87], v[140:143], v[132:135], v[84:87]\n\t"
  "v_mfma_f32_16x16x32_bf16 v[88:91], v[144:147], v[132:135], v[88:91]\n\t"
  "v_mfma_f32_16x16x32_bf16 v[92:95], v[148:151], v[132:135], v[92:95]\n\t"
  "v_mfma_f32_16x16x32_bf16 v[96:99], v[152:155], v[132:135], v[96:99]\n\t"
  "v_mfma_f32_16x16x32_bf16 v[100:103], v[156:159], v[132:135], v[100:103]\n\t"
  "v_mfma_f32_16x16x32_bf16 v[104:107], v[160:163], v[132:135], v[104:107]\n\t"
  "v_mfma_f32_16x16x32_bf16 v[108:111], v[164:167], v[132:135], v[108:111]\n\t"
  ".Lh1b_%=:\n\t"
  "s_setprio 0\n\t"
  "s_mov_b32 s61, s60\n\t"
  "s_add_u32 s60, s60, 32768\n\t"
  "s_and_b32 s60, s60, 0x1ffff\n\t"
  "s_add_u32 s68, s61, s83\n\t"
  "s_add_u32 m0, s68, 0x4000\n\t"
  "s_add_u32 s86, s63, s84\n\t"
  "buffer_load_dwordx4 v186, s[76:79], s86 offen offset:0 nt lds\n\t"
  "buffer_load_dwordx4 v187, s[76:79], s86 offen offset:1024 nt lds\n\t"
  "buffer_load_dwordx4 v186, s[76:79], s86 offen offset:2048 nt lds\n\t"
  "buffer_load_dwordx4 v187, s[76:79], s86 offen offset:3072 nt lds\n\t"
  "s_add_u32 s68, s61, s83\n\t"
  "s_add_u32 m0, s68, 0x5000\n\t"
  "s_add_u32 s86, s63, s85\n\t"
  "buffer_load_dwordx4 v186, s[76:79], s86 offen offset:0 nt lds\n\t"
  "buffer_load_dwordx4 v187, s[76:79], s86 offen offset:1024 nt lds\n\t"
  "buffer_load_dwordx4 v186, s[76:79], s86 offen offset:2048 nt lds\n\t"
  "buffer_load_dwordx4 v187, s[76:79], s86 offen offset:3072 nt lds\n\t"
  "s_add_u32 s68, s61, s83\n\t"
  "s_add_u32 m0, s68, 0x0\n\t"
  "s_add_u32 s86, s62, s84\n\t"
  "buffer_load_dwordx4 v253, s[72:75], s86 offen offset:0 nt lds\n\t"
  "buffer_load_dwordx4 v254, s[72:75], s86 offen offset:1024 nt lds\n\t"
  "buffer_load_dwordx4 v255, s[72:75], s86 offen offset:2048 nt lds\n\t"
  "buffer_load_dwordx4 v243, s[72:75], s86 offen offset:3072 nt lds\n\t"
  "s_add_u32 s68, s61, s83\n\t"
  "s_add_u32 m0, s68, 0x1000\n\t"
  "s_add_u32 s86, s62, s85\n\t"
  "buffer_load_dwordx4 v253, s[72:75], s86 offen offset:0 nt lds\n\t"
  "buffer_load_dwordx4 v254, s[72:75], s86 offen offset:1024 nt lds\n\t"
  "buffer_load_dwordx4 v255, s[72:75], s86 offen offset:2048 nt lds\n\t"
  "buffer_load_dwordx4 v243, s[72:75], s86 offen offset:3072 nt lds\n\t"
  "s_mov_b32 s63, s62\n\t"
  "s_add_u32 s62, s62, 0x4000\n\t"
  "s_and_b32 s62, s62, 0x3ffff\n\t"
  "v_add_u32_e32 v176, s60, v188\n\t"
  "ds_read_b128 v[136:139], v176 offset:0\n\t"
  "ds_read_b128 v[140:143], v176 offset:4096\n\t"
  "ds_read_b128 v[144:147], v176 offset:512\n\t"
  "ds_read_b128 v[148:151], v176 offset:4608\n\t"
  "ds_read_b128 v[152:155], v176 offset:1024\n\t"
  "ds_read_b128 v[156:159], v176 offset:5120\n\t"
  "ds_read_b128 v[160:163], v176 offset:1536\n\t"
  "ds_read_b128 v[164:167], v176 offset:5632\n\t"
  "s_sub_u32 s65, s65, 1\n\t"
  "s_cbranch_scc1 .Lht1_%=\n\t"
  "s_setprio 3\n\t"
  "s_waitcnt vmcnt(16) lgkmcnt(0)\n\t"
  "s_barrier\n\t"
  "v_mfma_f32_16x16x32_bf16 v[112:115], v[136:139], v[64:67], v[120:123]\n\t"
  "v_mfma_f32_16x16x32_bf16 v[112:115], v[144:147], v[68:71], v[112:115]\n\t"
  "v_mfma_f32_16x16x32_bf16 v[112:115], v[152:155], v[72:75], v[112:115]\n\t"
  "v_mfma_f32_16x16x32_bf16 v[112:115], v[160:163], v[76:79], v[112:115]\n\t"
  "v_mfma_f32_16x16x32_bf16 v[116:119], v[140:143], v[64:67], v[120:123]\n\t"
  "v_mfma_f32_16x16x32_bf16 v[116:119], v[148:151], v[68:71], v[116:119]\n\t"
  "v_mfma_f32_16x16x32_bf16 v[116:119], v[156:159], v[72:75], v[116:119]\n\t"
  "v_mfma_f32_16x16x32_bf16 v[116:119], v[164:167], v[76:79], v[116:119]\n\t"
  ".Lh2b_%=:\n\t"
  "s_setprio 0\n\t"
  "s_mov_b32 s61, s60\n\t"
  "s_add_u32 s60, s60, 32768\n\t"
  "s_and_b32 s60, s60, 0x1ffff\n\t"
  "s_mov_b32 s63, s62\n\t"
  "s_add_u32 s62, s62, 0x4000\n\t"
  "s_and_b32 s62, s62, 0x3ffff\n\t"
  "v_add_u32_e32 v177, s60, v189\n\t"
  "v_add_u32_e32 v178, s60, v179\n\t"
  "v_exp_f32_e32 v168, v112\n\t"
  "v_exp_f32_e32 v169, v113\n\t"
  "v_exp_f32_e32 v170, v114\n\t"
  "v_exp_f32_e32 v171, v115\n\t"
  "v_exp_f32_e32 v172, v116\n\t"
  "v_exp_f32_e32 v173, v117\n\t"
  "v_exp_f32_e32 v174, v118\n\t"
  "v_exp_f32_e32 v175, v119\n\t"
  "v_cvt_pk_bf16_f32 v132, v168, v169\n\t"
  "v_cvt_pk_bf16_f32 v133, v170, v171\n\t"
  "v_cvt_pk_bf16_f32 v134, v172, v173\n\t"
  "v_cvt_pk_bf16_f32 v135, v174, v175\n\t"
  "s_setprio 3\n\t"
  "s_nop 0\n\t"
  "v_mfma_f32_16x16x32_bf16 v[128:131], v[124:127], v[132:135], 0\n\t"
  "s_setprio 0\n\t"
  "ds_read_b64_tr_b16 v[136:137], v177 offset:0\n\t"
  "ds_read_b64_tr_b16 v[138:139], v177 offset:4096\n\t"
  "ds_read_b64_tr_b16 v[140:141], v178 offset:0\n\t"
  "ds_read_b64_tr_b16 v[142:143], v178 offset:4096\n\t"
  "ds_read_b64_tr_b16 v[144:145], v177 offset:512\n\t"
  "ds_read_b64_tr_b16 v[146:147], v177 offset:4608\n\t"
  "ds_read_b64_tr_b16 v[148:149], v178 offset:512\n\t"
  "ds_read_b64_tr_b16 v[150:151], v178 offset:4608\n\t"
  "ds_read_b64_tr_b16 v[152:153], v177 offset:1024\n\t"
  "ds_read_b64_tr_b16 v[154:155], v177 offset:5120\n\t"
  "ds_read_b64_tr_b16 v[156:157], v178 offset:1024\n\t"
  "ds_read_b64_tr_b16 v[158:159], v178 offset:5120\n\t"
  "ds_read_b64_tr_b16 v[160:161], v177 offset:1536\n\t"
  "ds_read_b64_tr_b16 v[162:163], v177 offset:5632\n\t"
  "ds_read_b64_tr_b16 v[164:165], v178 offset:1536\n\t"
  "ds_read_b64_tr_b16 v[166:167], v178 offset:5632\n\t"
  "v_cmp_nge_f32_e32 vcc, 0x4e800000, v128\n\t"
  "s_cbranch_vccnz .Lhrare0_%=\n\t"
  ".Lhret0_%=:\n\t"
  "v_add_f32_e32 v181, v181, v128\n\t"
  "s_sub_u32 s65, s65, 1\n\t"
  "s_cbranch_scc1 .Lht0_%=\n\t"
  "s_branch .Lh1pre_%=\n\t"
  ".Lht1_%=:\n\t"
  "s_setprio 3\n\t"
  "s_waitcnt vmcnt(16) lgkmcnt(0)\n\t"
  "s_barrier\n\t"
  "v_mfma_f32_16x16x32_bf16 v[112:115], v[136:139], v[64:67], v[120:123]\n\t"
  "v_mfma_f32_16x16x32_bf16 v[112:115], v[144:147], v[68:71], v[112:115]\n\t"
  "v_mfma_f32_16x16x32_bf16 v[112:115], v[152:155], v[72:75], v[112:115]\n\t"
  "v_mfma_f32_16x16x32_bf16 v[112:115], v[160:163], v[76:79], v[112:115]\n\t"
  "v_mfma_f32_16x16x32_bf16 v[116:119], v[140:143], v[64:67], v[120:123]\n\t"
  "v_mfma_f32_16x16x32_bf16 v[116:119], v[148:151], v[68:71], v[116:119]\n\t"
  "v_mfma_f32_16x16x32_bf16 v[116:119], v[156:159], v[72:75], v[116:119]\n\t"
  "v_mfma_f32_16x16x32_bf16 v[116:119], v[164:167], v[76:79], v[116:119]\n\t"
  "s_setprio 0\n\t"
  "s_mov_b32 s61, s60\n\t"
  "s_add_u32 s60, s60, 32768\n\t"
  "s_and_b32 s60, s60, 0x1ffff\n\t"
  "s_mov_b32 s63, s62\n\t"
  "s_add_u32 s62, s62, 0x4000\n\t"
  "s_and_b32 s62, s62, 0x3ffff\n\t"
  "v_add_u32_e32 v177, s60, v189\n\t"
  "v_add_u32_e32 v178, s60, v179\n\t"
  "v_exp_f32_e32 v168, v112\n\t"
  "v_exp_f32_e32 v169, v113\n\t"
  "v_exp_f32_e32 v170, v114\n\t"
  "v_exp_f32_e32 v171, v115\n\t"
  "v_exp_f32_e32 v172, v116\n\t"
  "v_exp_f32_e32 v173, v117\n\t"
  "v_exp_f32_e32 v174, v118\n\t"
  "v_exp_f32_e32 v175, v119\n\t"
  "v_cvt_pk_bf16_f32 v132, v168, v169\n\t"
  "v_cvt_pk_bf16_f32 v133, v170, v171\n\t"
  "v_cvt_pk_bf16_f32 v134, v172, v173\n\t"
  "v_cvt_pk_bf16_f32 v135, v174, v175\n\t"
  "s_setprio 3\n\t"
  "s_nop 0\n\t"
  "v_mfma_f32_16x16x32_bf16 v[128:131], v[124:127], v[132:135], 0\n\t"
  "s_setprio 0\n\t"
  "ds_read_b64_tr_b16 v[136:137], v177 offset:0\n\t"
  "ds_read_b64_tr_b16 v[138:139], v177 offset:4096\n\t"
  "ds_read_b64_tr_b16 v[140:141], v178 offset:0\n\t"
  "ds_read_b64_tr_b16 v[142:143], v178 offset:4096\n\t"
  "ds_read_b64_tr_b16 v[144:145], v177 offset:512\n\t"
  "ds_read_b64_tr_b16 v[146:147], v177 offset:4608\n\t"
  "ds_read_b64_tr_b16 v[148:149], v178 offset:512\n\t"
  "ds_read_b64_tr_b16 v[150:151], v178 offset:4608\n\t"
  "ds_read_b64_tr_b16 v[152:153], v177 offset:1024\n\t"
  "ds_read_b64_tr_b16 v[154:155], v177 offset:5120\n\t"
  "ds_read_b64_tr_b16 v[156:157], v178 offset:1024\n\t"
  "ds_read_b64_tr_b16 v[158:159], v178 offset:5120\n\t"
  "ds_read_b64_tr_b16 v[160:161], v177 offset:1536\n\t"
  "ds_read_b64_tr_b16 v[162:163], v177 offset:5632\n\t"
  "ds_read_b64_tr_b16 v[164:165], v178 offset:1536\n\t"
  "ds_read_b64_tr_b16 v[166:167], v178 offset:5632\n\t"
  "v_cmp_nge_f32_e32 vcc, 0x4e800000, v128\n\t"
  "s_cbranch_vccnz .Lhrare1_%=\n\t"
  ".Lhret1_%=:\n\t"
  "v_add_f32_e32 v181, v181, v128\n\t"
  ".Lht0_%=:\n\t"
  "s_setprio 3\n\t"
  "s_waitcnt vmcnt(8) lgkmcnt(0)\n\t"
  "s_barrier\n\t"
  "v_mfma_f32_16x16x32_bf16 v[80:83], v[136:139], v[132:135], v[80:83]\n\t"
  "v_mfma_f32_16x16x32_bf16 v[84:87], v[140:143], v[132:135], v[84:87]\n\t"
  "v_mfma_f32_16x16x32_bf16 v[88:91], v[144:147], v[132:135], v[88:91]\n\t"
  "v_mfma_f32_16x16x32_bf16 v[92:95], v[148:151], v[132:135], v[92:95]\n\t"
  "v_mfma_f32_16x16x32_bf16 v[96:99], v[152:155], v[132:135], v[96:99]\n\t"
  "v_mfma_f32_16x16x32_bf16 v[100:103], v[156:159], v[132:135], v[100:103]\n\t"
  "v_mfma_f32_16x16x32_bf16 v[104:107], v[160:163], v[132:135], v[104:107]\n\t"
  "v_mfma_f32_16x16x32_bf16 v[108:111], v[164:167], v[132:135], v[108:111]\n\t"
  "s_setprio 0\n\t"
  "s_mov_b32 s61, s60\n\t"
  "s_add_u32 s60, s60, 32768\n\t"
  "s_and_b32 s60, s60, 0x1ffff\n\t"
  "v_add_u32_e32 v176, s60, v188\n\t"
  "ds_read_b128 v[136:139], v176 offset:0\n\t"
  "ds_read_b128 v[140:143], v176 offset:4096\n\t"
  "ds_read_b128 v[144:147], v176 offset:512\n\t"
  "ds_read_b128 v[148:151], v176 offset:4608\n\t"
  "ds_read_b128 v[152:155], v176 offset:1024\n\t"
  "ds_read_b128 v[156:159], v176 offset:5120\n\t"
  "ds_read_b128 v[160:163], v176 offset:1536\n\t"
  "ds_read_b128 v[164:167], v176 offset:5632\n\t"
  "s_setprio 3\n\t"
  "s_waitcnt vmcnt(0) lgkmcnt(0)\n\t"
  "s_barrier\n\t"
  "v_mfma_f32_16x16x32_bf16 v[112:115], v[136:139], v[64:67], v[120:123]\n\t"
  "v_mfma_f32_16x16x32_bf16 v[112:115], v[144:147], v[68:71], v[112:115]\n\t"
  "v_mfma_f32_16x16x32_bf16 v[112:115], v[152:155], v[72:75], v[112:115]\n\t"
  "v_mfma_f32_16x16x32_bf16 v[112:115], v[160:163], v[76:79], v[112:115]\n\t"
  "v_mfma_f32_16x16x32_bf16 v[116:119], v[140:143], v[64:67], v[120:123]\n\t"
  "v_mfma_f32_16x16x32_bf16 v[116:119], v[148:151], v[68:71], v[116:119]\n\t"
  "v_mfma_f32_16x16x32_bf16 v[116:119], v[156:159], v[72:75], v[116:119]\n\t"
  "v_mfma_f32_16x16x32_bf16 v[116:119], v[164:167], v[76:79], v[116:119]\n\t"
  "s_setprio 0\n\t"
  "s_mov_b32 s61, s60\n\t"
  "s_add_u32 s60, s60, 32768\n\t"
  "s_and_b32 s60, s60, 0x1ffff\n\t"
  "s_mov_b32 s63, s62\n\t"
  "s_add_u32 s62, s62, 0x4000\n\t"
  "s_and_b32 s62, s62, 0x3ffff\n\t"
  "v_add_u32_e32 v177, s60, v189\n\t"
  "v_add_u32_e32 v178, s60, v179\n\t"
  "v_exp_f32_e32 v168, v112\n\t"
  "v_exp_f32_e32 v169, v113\n\t"
  "v_exp_f32_e32 v170, v114\n\t"
  "v_exp_f32_e32 v171, v115\n\t"
  "v_exp_f32_e32 v172, v116\n\t"
  "v_exp_f32_e32 v173, v117\n\t"
  "v_exp_f32_e32 v174, v118\n\t"
  "v_exp_f32_e32 v175, v119\n\t"
  "v_cvt_pk_bf16_f32 v132, v168, v169\n\t"
  "v_cvt_pk_bf16_f32 v133, v170, v171\n\t"
  "v_cvt_pk_bf16_f32 v134, v172, v173\n\t"
  "v_cvt_pk_bf16_f32 v135, v174, v175\n\t"
  "s_setprio 3\n\t"
  "s_nop 0\n\t"
  "v_mfma_f32_16x16x32_bf16 v[128:131], v[124:127], v[132:135], 0\n\t"
  "s_setprio 0\n\t"
  "ds_read_b64_tr_b16 v[136:137], v177 offset:0\n\t"
  "ds_read_b64_tr_b16 v[138:139], v177 offset:4096\n\t"
  "ds_read_b64_tr_b16 v[140:141], v178 offset:0\n\t"
  "ds_read_b64_tr_b16 v[142:143], v178 offset:4096\n\t"
  "ds_read_b64_tr_b16 v[144:145], v177 offset:512\n\t"
  "ds_read_b64_tr_b16 v[146:147], v177 offset:4608\n\t"
  "ds_read_b64_tr_b16 v[148:149], v178 offset:512\n\t"
  "ds_read_b64_tr_b16 v[150:151], v178 offset:4608\n\t"
  "ds_read_b64_tr_b16 v[152:153], v177 offset:1024\n\t"
  "ds_read_b64_tr_b16 v[154:155], v177 offset:5120\n\t"
  "ds_read_b64_tr_b16 v[156:157], v178 offset:1024\n\t"
  "ds_read_b64_tr_b16 v[158:159], v178 offset:5120\n\t"
  "ds_read_b64_tr_b16 v[160:161], v177 offset:1536\n\t"
  "ds_read_b64_tr_b16 v[162:163], v177 offset:5632\n\t"
  "ds_read_b64_tr_b16 v[164:165], v178 offset:1536\n\t"
  "ds_read_b64_tr_b16 v[166:167], v178 offset:5632\n\t"
  "v_cmp_nge_f32_e32 vcc, 0x4e800000, v128\n\t"
  "s_cbranch_vccnz .Lhrare2_%=\n\t"
  ".Lhret2_%=:\n\t"
  "v_add_f32_e32 v181, v181, v128\n\t"
  "s_cmp_lg_u32 s81, 0\n\t"
  "s_setprio 3\n\t"
  "s_waitcnt lgkmcnt(0)\n\t"
  "s_cbranch_scc1 .Lhnb_%=\n\t"
  "s_setprio 3\n\t"
  "s_barrier\n\t"
  ".Lhnb_%=:\n\t"
  "v_mfma_f32_16x16x32_bf16 v[80:83], v[136:139], v[132:135], v[80:83]\n\t"
  "v_mfma_f32_16x16x32_bf16 v[84:87], v[140:143], v[132:135], v[84:87]\n\t"
  "v_mfma_f32_16x16x32_bf16 v[88:91], v[144:147], v[132:135], v[88:91]\n\t"
  "v_mfma_f32_16x16x32_bf16 v[92:95], v[148:151], v[132:135], v[92:95]\n\t"
  "v_mfma_f32_16x16x32_bf16 v[96:99], v[152:155], v[132:135], v[96:99]\n\t"
  "v_mfma_f32_16x16x32_bf16 v[100:103], v[156:159], v[132:135], v[100:103]\n\t"
  "v_mfma_f32_16x16x32_bf16 v[104:107], v[160:163], v[132:135], v[104:107]\n\t"
  "v_mfma_f32_16x16x32_bf16 v[108:111], v[164:167], v[132:135], v[108:111]\n\t"
  "s_setprio 0\n\t"
  "s_mul_i32 s49, s82, 8448\n\t"
  "s_add_u32 s49, s49, %3\n\t"
  "s_lshl_b32 s50, s82, 7\n\t"
  "s_add_u32 s50, s50, %3\n\t"
  "s_add_u32 s50, s50, 33792\n\t"
  "v_mul_u32_u24_e32 v190, 0x210, v252\n\t"
  "v_lshl_add_u32 v190, v251, 4, v190\n\t"
  "v_add_u32_e32 v190, s49, v190\n\t"
  "v_lshl_add_u32 v191, v252, 3, s50\n\t"
  "v_sub_f32_e32 v180, 0, v120\n\t"
  "ds_write_b128 v190, v[80:83] offset:0\n\t"
  "ds_write_b128 v190, v[84:87] offset:64\n\t"
  "ds_write_b128 v190, v[88:91] offset:128\n\t"
  "ds_write_b128 v190, v[92:95] offset:192\n\t"
  "ds_write_b128 v190, v[96:99] offset:256\n\t"
  "ds_write_b128 v190, v[100:103] offset:320\n\t"
  "ds_write_b128 v190, v[104:107] offset:384\n\t"
  "ds_write_b128 v190, v[108:111] offset:448\n\t"
  "ds_write_b64 v191, v[180:181]\n\t"
  "s_lshl_b32 s49, s82, 2\n\t"
  "v_add_u32_e32 v182, s49, v251\n\t"
  "v_mul_u32_u24_e32 v183, 0x210, v182\n\t"
  "v_lshl_add_u32 v183, v252, 5, v183\n\t"
  "v_add_u32_e32 v183, %3, v183\n\t"
  "v_lshl_add_u32 v182, v182, 3, %3\n\t"
  "s_lshl_b32 s49, s82, 10\n\t"
  "s_add_u32 s49, s49, 0x8000\n\t"
  "v_lshl_add_u32 v176, v250, 4, s49\n\t"
  "v_mov_b32_e32 v184, 1\n\t"
  "s_waitcnt lgkmcnt(0)\n\t"
  "ds_add_u32 v194, v184\n\t"
  "s_setprio 0\n\t"
  ".Lhspin_%=:\n\t"
  "s_sleep 1\n\t"
  "ds_read_b32 v184, v194\n\t"
  "s_waitcnt lgkmcnt(0)\n\t"
  "v_cmp_gt_u32_e32 vcc, 0x100, v184\n\t"
  "s_cbranch_vccnz .Lhspin_%=\n\t"
  "ds_read_b64 v[168:169], v182 offset:33792\n\t"
  "ds_read_b64 v[170:171], v182 offset:33920\n\t"
  "ds_read_b64 v[172:173], v182 offset:34048\n\t"
  "ds_read_b64 v[174:175], v182 offset:34176\n\t"
  "ds_read_b128 v[136:139], v183 offset:0\n\t"
  "ds_read_b128 v[140:143], v183 offset:16\n\t"
  "ds_read_b128 v[144:147], v183 offset:8448\n\t"
  "ds_read_b128 v[148:151], v183 offset:8464\n\t"
  "ds_read_b128 v[152:155], v183 offset:16896\n\t"
  "ds_read_b128 v[156:159], v183 offset:16912\n\t"
  "ds_read_b128 v[160:163], v183 offset:25344\n\t"
  "ds_read_b128 v[164:167], v183 offset:25360\n\t"
  "s_waitcnt lgkmcnt(8)\n\t"
  "v_max3_f32 v184, v168, v170, v172\n\t"
  "v_max_f32_e32 v184, v184, v174\n\t"
  "v_sub_f32_e32 v168, v168, v184\n\t"
  "v_sub_f32_e32 v170, v170, v184\n\t"
  "v_sub_f32_e32 v172, v172, v184\n\t"
  "v_sub_f32_e32 v174, v174, v184\n\t"
  "v_exp_f32_e32 v168, v168\n\t"
  "v_exp_f32_e32 v170, v170\n\t"
  "v_exp_f32_e32 v172, v172\n\t"
  "v_exp_f32_e32 v174, v174\n\t"
  "v_mul_f32_e32 v185, v168, v169\n\t"
  "v_fmac_f32_e32 v185, v170, v171\n\t"
  "v_fmac_f32_e32 v185, v172, v173\n\t"
  "v_fmac_f32_e32 v185, v174, v175\n\t"
  "v_rcp_f32_e32 v185, v185\n\t"
  "s_waitcnt lgkmcnt(0)\n\t"
  "v_mul_f32_e32 v168, v168, v185\n\t"
  "v_mul_f32_e32 v170, v170, v185\n\t"
  "v_mul_f32_e32 v172, v172, v185\n\t"
  "v_mul_f32_e32 v174, v174, v185\n\t"
  "v_mul_f32_e32 v80, v168, v136\n\t"
  "v_mul_f32_e32 v81, v168, v137\n\t"
  "v_mul_f32_e32 v82, v168, v138\n\t"
  "v_mul_f32_e32 v83, v168, v139\n\t"
  "v_mul_f32_e32 v84, v168, v140\n\t"
  "v_mul_f32_e32 v85, v168, v141\n\t"
  "v_mul_f32_e32 v86, v168, v142\n\t"
  "v_mul_f32_e32 v87, v168, v143\n\t"
  "v_fmac_f32_e32 v80, v170, v144\n\t"
  "v_fmac_f32_e32 v81, v170, v145\n\t"
  "v_fmac_f32_e32 v82, v170, v146\n\t"
  "v_fmac_f32_e32 v83, v170, v147\n\t"
  "v_fmac_f32_e32 v84, v170, v148\n\t"
  "v_fmac_f32_e32 v85, v170, v149\n\t"
  "v_fmac_f32_e32 v86, v170, v150\n\t"
  "v_fmac_f32_e32 v87, v170, v151\n\t"
  "v_fmac_f32_e32 v80, v172, v152\n\t"
  "v_fmac_f32_e32 v81, v172, v153\n\t"
  "v_fmac_f32_e32 v82, v172, v154\n\t"
  "v_fmac_f32_e32 v83, v172, v155\n\t"
  "v_fmac_f32_e32 v84, v172, v156\n\t"
  "v_fmac_f32_e32 v85, v172, v157\n\t"
  "v_fmac_f32_e32 v86, v172, v158\n\t"
  "v_fmac_f32_e32 v87, v172, v159\n\t"
  "v_fmac_f32_e32 v80, v174, v160\n\t"
  "v_fmac_f32_e32 v81, v174, v161\n\t"
  "v_fmac_f32_e32 v82, v174, v162\n\t"
  "v_fmac_f32_e32 v83, v174, v163\n\t"
  "v_fmac_f32_e32 v84, v174, v164\n\t"
  "v_fmac_f32_e32 v85, v174, v165\n\t"
  "v_fmac_f32_e32 v86, v174, v166\n\t"
  "v_fmac_f32_e32 v87, v174, v167\n\t"
  "v_cvt_pk_bf16_f32 v132, v80, v81\n\t"
  "v_cvt_pk_bf16_f32 v133, v82, v83\n\t"
  "v_cvt_pk_bf16_f32 v134, v84, v85\n\t"
  "v_cvt_pk_bf16_f32 v135, v86, v87\n\t"
  "global_store_dwordx4 v176, v[132:135], s[58:59] nt\n\t"
  "s_endpgm\n\t"
  ".Lhot_end_%=:\n\t"
  ".Lrare0_%=:\n\t"
  "s_nop 7\n\t"
  "v_max3_f32 v238, v112, v113, v114\n\t"
  "v_max3_f32 v239, v116, v117, v118\n\t"
  "v_max3_f32 v238, v238, v115, v120\n\t"
  "v_max3_f32 v239, v239, v119, v124\n\t"
  "v_max3_f32 v238, v238, v121, v122\n\t"
  "v_max3_f32 v239, v239, v125, v126\n\t"
  "v_max_f32_e32 v238, v238, v123\n\t"
  "v_max_f32_e32 v239, v239, v127\n\t"
  "v_mov_b32_e32 v240, v238\n\t"
  "v_mov_b32_e32 v241, v239\n\t"
  "s_nop 0\n\t"
  "v_permlane16_swap_b32_e32 v238, v240\n\t"
  "v_permlane16_swap_b32_e32 v239, v241\n\t"
  "v_max_f32_e32 v238, v238, v240\n\t"
  "v_max_f32_e32 v239, v239, v241\n\t"
  "v_mov_b32_e32 v240, v238\n\t"
  "v_mov_b32_e32 v241, v239\n\t"
  "s_nop 0\n\t"
  "v_permlane32_swap_b32_e32 v238, v240\n\t"
  "v_permlane32_swap_b32_e32 v239, v241\n\t"
  "v_max3_f32 v238, v238, v240, 0\n\t"
  "v_max3_f32 v239, v239, v241, 0\n\t"
  "v_exp_f32_e64 v240, -v238\n\t"
  "v_exp_f32_e64 v241, -v239\n\t"
  "v_sub_f32_e32 v112, v112, v238\n\t"
  "v_sub_f32_e32 v113, v113, v238\n\t"
  "v_sub_f32_e32 v114, v114, v238\n\t"
  "v_sub_f32_e32 v115, v115, v238\n\t"
  "v_sub_f32_e32 v120, v120, v238\n\t"
  "v_sub_f32_e32 v121, v121, v238\n\t"
  "v_sub_f32_e32 v122, v122, v238\n\t"
  "v_sub_f32_e32 v123, v123, v238\n\t"
  "v_sub_f32_e32 v116, v116, v239\n\t"
  "v_sub_f32_e32 v117, v117, v239\n\t"
  "v_sub_f32_e32 v118, v118, v239\n\t"
  "v_sub_f32_e32 v119, v119, v239\n\t"
  "v_sub_f32_e32 v124, v124, v239\n\t"
  "v_sub_f32_e32 v125, v125, v239\n\t"
  "v_sub_f32_e32 v126, v126, v239\n\t"
  "v_sub_f32_e32 v127, v127, v239\n\t"
  "v_sub_f32_e32 v96, v96, v238\n\t"
  "v_sub_f32_e32 v97, v97, v238\n\t"
  "v_sub_f32_e32 v98, v98, v238\n\t"
  "v_sub_f32_e32 v99, v99, v238\n\t"
  "v_sub_f32_e32 v104, v104, v238\n\t"
  "v_sub_f32_e32 v105, v105, v238\n\t"
  "v_sub_f32_e32 v106, v106, v238\n\t"
  "v_sub_f32_e32 v107, v107, v238\n\t"
  "v_sub_f32_e32 v100, v100, v239\n\t"
  "v_sub_f32_e32 v101, v101, v239\n\t"
  "v_sub_f32_e32 v102, v102, v239\n\t"
  "v_sub_f32_e32 v103, v103, v239\n\t"
  "v_sub_f32_e32 v108, v108, v239\n\t"
  "v_sub_f32_e32 v109, v109, v239\n\t"
  "v_sub_f32_e32 v110, v110, v239\n\t"
  "v_sub_f32_e32 v111, v111, v239\n\t"
  "v_sub_f32_e32 v128, v128, v238\n\t"
  "v_sub_f32_e32 v129, v129, v238\n\t"
  "v_sub_f32_e32 v130, v130, v238\n\t"
  "v_sub_f32_e32 v131, v131, v238\n\t"
  "v_sub_f32_e32 v132, v132, v239\n\t"
  "v_sub_f32_e32 v133, v133, v239\n\t"
  "v_sub_f32_e32 v134, v134, v239\n\t"
  "v_sub_f32_e32 v135, v135, v239\n\t"
  "v_mul_f32_e32 v0, v0, v240\n\t"
  "v_mul_f32_e32 v1, v1, v240\n\t"
  "v_mul_f32_e32 v2, v2, v240\n\t"
  "v_mul_f32_e32 v3, v3, v240\n\t"
  "v_mul_f32_e32 v4, v4, v241\n\t"
  "v_mul_f32_e32 v5, v5, v241\n\t"
  "v_mul_f32_e32 v6, v6, v241\n\t"
  "v_mul_f32_e32 v7, v7, v241\n\t"
  "v_mul_f32_e32 v8, v8, v240\n\t"
  "v_mul_f32_e32 v9, v9, v240\n\t"
  "v_mul_f32_e32 v10, v10, v240\n\t"
  "v_mul_f32_e32 v11, v11, v240\n\t"
  "v_mul_f32_e32 v12, v12, v241\n\t"
  "v_mul_f32_e32 v13, v13, v241\n\t"
  "v_mul_f32_e32 v14, v14, v241\n\t"
  "v_mul_f32_e32 v15, v15, v241\n\t"
  "v_mul_f32_e32 v16, v16, v240\n\t"
  "v_mul_f32_e32 v17, v17, v240\n\t"
  "v_mul_f32_e32 v18, v18, v240\n\t"
  "v_mul_f32_e32 v19, v19, v240\n\t"
  "v_mul_f32_e32 v20, v20, v241\n\t"
  "v_mul_f32_e32 v21, v21, v241\n\t"
  "v_mul_f32_e32 v22, v22, v241\n\t"
  "v_mul_f32_e32 v23, v23, v241\n\t"
  "v_mul_f32_e32 v24, v24, v240\n\t"
  "v_mul_f32_e32 v25, v25, v240\n\t"
  "v_mul_f32_e32 v26, v26, v240\n\t"
  "v_mul_f32_e32 v27, v27, v240\n\t"
  "v_mul_f32_e32 v28, v28, v241\n\t"
  "v_mul_f32_e32 v29, v29, v241\n\t"
  "v_mul_f32_e32 v30, v30, v241\n\t"
  "v_mul_f32_e32 v31, v31, v241\n\t"
  "v_mul_f32_e32 v32, v32, v240\n\t"
  "v_mul_f32_e32 v33, v33, v240\n\t"
  "v_mul_f32_e32 v34, v34, v240\n\t"
  "v_mul_f32_e32 v35, v35, v240\n\t"
  "v_mul_f32_e32 v36, v36, v241\n\t"
  "v_mul_f32_e32 v37, v37, v241\n\t"
  "v_mul_f32_e32 v38, v38, v241\n\t"
  "v_mul_f32_e32 v39, v39, v241\n\t"
  "v_mul_f32_e32 v40, v40, v240\n\t"
  "v_mul_f32_e32 v41, v41, v240\n\t"
  "v_mul_f32_e32 v42, v42, v240\n\t"
  "v_mul_f32_e32 v43, v43, v240\n\t"
  "v_mul_f32_e32 v44, v44, v241\n\t"
  "v_mul_f32_e32 v45, v45, v241\n\t"
  "v_mul_f32_e32 v46, v46, v241\n\t"
  "v_mul_f32_e32 v47, v47, v241\n\t"
  "v_mul_f32_e32 v48, v48, v240\n\t"
  "v_mul_f32_e32 v49, v49, v240\n\t"
  "v_mul_f32_e32 v50, v50, v240\n\t"
  "v_mul_f32_e32 v51, v51, v240\n\t"
  "v_mul_f32_e32 v52, v52, v241\n\t"
  "v_mul_f32_e32 v53, v53, v241\n\t"
  "v_mul_f32_e32 v54, v54, v241\n\t"
  "v_mul_f32_e32 v55, v55, v241\n\t"
  "v_mul_f32_e32 v56, v56, v240\n\t"
  "v_mul_f32_e32 v57, v57, v240\n\t"
  "v_mul_f32_e32 v58, v58, v240\n\t"
  "v_mul_f32_e32 v59, v59, v240\n\t"
  "v_mul_f32_e32 v60, v60, v241\n\t"
  "v_mul_f32_e32 v61, v61, v241\n\t"
  "v_mul_f32_e32 v62, v62, v241\n\t"
  "v_mul_f32_e32 v63, v63, v241\n\t"
  "v_mul_f32_e32 v236, v236, v240\n\t"
  "v_mul_f32_e32 v237, v237, v241\n\t"
  "v_exp_f32_e32 v228, v112\n\t"
  "v_exp_f32_e32 v229, v113\n\t"
  "v_exp_f32_e32 v230, v114\n\t"
  "v_exp_f32_e32 v231, v115\n\t"
  "v_cvt_pk_bf16_f32 v156, v228, v229\n\t"
  "v_exp_f32_e32 v232, v116\n\t"
  "v_exp_f32_e32 v233, v117\n\t"
  "v_cvt_pk_bf16_f32 v157, v230, v231\n\t"
  "v_exp_f32_e32 v234, v118\n\t"
  "v_exp_f32_e32 v235, v119\n\t"
  "v_cvt_pk_bf16_f32 v160, v232, v233\n\t"
  "v_exp_f32_e32 v228, v120\n\t"
  "v_exp_f32_e32 v229, v121\n\t"
  "v_cvt_pk_bf16_f32 v161, v234, v235\n\t"
  "v_exp_f32_e32 v230, v122\n\t"
  "v_exp_f32_e32 v231, v123\n\t"
  "v_cvt_pk_bf16_f32 v158, v228, v229\n\t"
  "v_exp_f32_e32 v232, v124\n\t"
  "v_exp_f32_e32 v233, v125\n\t"
  "v_cvt_pk_bf16_f32 v159, v230, v231\n\t"
  "v_exp_f32_e32 v234, v126\n\t"
  "v_exp_f32_e32 v235, v127\n\t"
  "v_cvt_pk_bf16_f32 v162, v232, v233\n\t"
  "s_nop 0\n\t"
  "v_cvt_pk_bf16_f32 v163, v234, v235\n\t"
  "s_nop 1\n\t"
  "v_mfma_f32_16x16x32_bf16 v[140:143], v[136:139], v[156:159], 0\n\t"
  "v_mfma_f32_16x16x32_bf16 v[144:147], v[136:139], v[160:163], 0\n\t"
  "v_exp_f32_e32 v228, v96\n\t"
  "v_exp_f32_e32 v229, v97\n\t"
  "v_exp_f32_e32 v230, v98\n\t"
  "s_nop 7\n\t"
  "s_branch .Lret0_%=\n\t"
  ".Lrare1_%=:\n\t"
  "s_nop 7\n\t"
  "v_max3_f32 v238, v96, v97, v98\n\t"
  "v_max3_f32 v239, v100, v101, v102\n\t"
  "v_max3_f32 v238, v238, v99, v104\n\t"
  "v_max3_f32 v239, v239, v103, v108\n\t"
  "v_max3_f32 v238, v238, v105, v106\n\t"
  "v_max3_f32 v239, v239, v109, v110\n\t"
  "v_max_f32_e32 v238, v238, v107\n\t"
  "v_max_f32_e32 v239, v239, v111\n\t"
  "v_mov_b32_e32 v240, v238\n\t"
  "v_mov_b32_e32 v241, v239\n\t"
  "s_nop 0\n\t"
  "v_permlane16_swap_b32_e32 v238, v240\n\t"
  "v_permlane16_swap_b32_e32 v239, v241\n\t"
  "v_max_f32_e32 v238, v238, v240\n\t"
  "v_max_f32_e32 v239, v239, v241\n\t"
  "v_mov_b32_e32 v240, v238\n\t"
  "v_mov_b32_e32 v241, v239\n\t"
  "s_nop 0\n\t"
  "v_permlane32_swap_b32_e32 v238, v240\n\t"
  "v_permlane32_swap_b32_e32 v239, v241\n\t"
  "v_max3_f32 v238, v238, v240, 0\n\t"
  "v_max3_f32 v239, v239, v241, 0\n\t"
  "v_exp_f32_e64 v240, -v238\n\t"
  "v_exp_f32_e64 v241, -v239\n\t"
  "v_sub_f32_e32 v96, v96, v238\n\t"
  "v_sub_f32_e32 v97, v97, v238\n\t"
  "v_sub_f32_e32 v98, v98, v238\n\t"
  "v_sub_f32_e32 v99, v99, v238\n\t"
  "v_sub_f32_e32 v104, v104, v238\n\t"
  "v_sub_f32_e32 v105, v105, v238\n\t"
  "v_sub_f32_e32 v106, v106, v238\n\t"
  "v_sub_f32_e32 v107, v107, v238\n\t"
  "v_sub_f32_e32 v100, v100, v239\n\t"
  "v_sub_f32_e32 v101, v101, v239\n\t"
  "v_sub_f32_e32 v102, v102, v239\n\t"
  "v_sub_f32_e32 v103, v103, v239\n\t"
  "v_sub_f32_e32 v108, v108, v239\n\t"
  "v_sub_f32_e32 v109, v109, v239\n\t"
  "v_sub_f32_e32 v110, v110, v239\n\t"
  "v_sub_f32_e32 v111, v111, v239\n\t"
  "v_sub_f32_e32 v112, v112, v238\n\t"
  "v_sub_f32_e32 v113, v113, v238\n\t"
  "v_sub_f32_e32 v114, v114, v238\n\t"
  "v_sub_f32_e32 v115, v115, v238\n\t"
  "v_sub_f32_e32 v120, v120, v238\n\t"
  "v_sub_f32_e32 v121, v121, v238\n\t"
  "v_sub_f32_e32 v122, v122, v238\n\t"
  "v_sub_f32_e32 v123, v123, v238\n\t"
  "v_sub_f32_e32 v116, v116, v239\n\t"
  "v_sub_f32_e32 v117, v117, v239\n\t"
  "v_sub_f32_e32 v118, v118, v239\n\t"
  "v_sub_f32_e32 v119, v119, v239\n\t"
  "v_sub_f32_e32 v124, v124, v239\n\t"
  "v_sub_f32_e32 v125, v125, v239\n\t"
  "v_sub_f32_e32 v126, v126, v239\n\t"
  "v_sub_f32_e32 v127, v127, v239\n\t"
  "v_sub_f32_e32 v128, v128, v238\n\t"
  "v_sub_f32_e32 v129, v129, v238\n\t"
  "v_sub_f32_e32 v130, v130, v238\n\t"
  "v_sub_f32_e32 v131, v131, v238\n\t"
  "v_sub_f32_e32 v132, v132, v239\n\t"
  "v_sub_f32_e32 v133, v133, v239\n\t"
  "v_sub_f32_e32 v134, v134, v239\n\t"
  "v_sub_f32_e32 v135, v135, v239\n\t"
  "v_mul_f32_e32 v0, v0, v240\n\t"
  "v_mul_f32_e32 v1, v1, v240\n\t"
  "v_mul_f32_e32 v2, v2, v240\n\t"
  "v_mul_f32_e32 v3, v3, v240\n\t"
  "v_mul_f32_e32 v4, v4, v241\n\t"
  "v_mul_f32_e32 v5, v5, v241\n\t"
  "v_mul_f32_e32 v6, v6, v241\n\t"
  "v_mul_f32_e32 v7, v7, v241\n\t"
  "v_mul_f32_e32 v8, v8, v240\n\t"
  "v_mul_f32_e32 v9, v9, v240\n\t"
  "v_mul_f32_e32 v10, v10, v240\n\t"
  "v_mul_f32_e32 v11, v11, v240\n\t"
  "v_mul_f32_e32 v12, v12, v241\n\t"
  "v_mul_f32_e32 v13, v13, v241\n\t"
  "v_mul_f32_e32 v14, v14, v241\n\t"
  "v_mul_f32_e32 v15, v15, v241\n\t"
  "v_mul_f32_e32 v16, v16, v240\n\t"
  "v_mul_f32_e32 v17, v17, v240\n\t"
  "v_mul_f32_e32 v18, v18, v240\n\t"
  "v_mul_f32_e32 v19, v19, v240\n\t"
  "v_mul_f32_e32 v20, v20, v241\n\t"
  "v_mul_f32_e32 v21, v21, v241\n\t"
  "v_mul_f32_e32 v22, v22, v241\n\t"
  "v_mul_f32_e32 v23, v23, v241\n\t"
  "v_mul_f32_e32 v24, v24, v240\n\t"
  "v_mul_f32_e32 v25, v25, v240\n\t"
  "v_mul_f32_e32 v26, v26, v240\n\t"
  "v_mul_f32_e32 v27, v27, v240\n\t"
  "v_mul_f32_e32 v28, v28, v241\n\t"
  "v_mul_f32_e32 v29, v29, v241\n\t"
  "v_mul_f32_e32 v30, v30, v241\n\t"
  "v_mul_f32_e32 v31, v31, v241\n\t"
  "v_mul_f32_e32 v32, v32, v240\n\t"
  "v_mul_f32_e32 v33, v33, v240\n\t"
  "v_mul_f32_e32 v34, v34, v240\n\t"
  "v_mul_f32_e32 v35, v35, v240\n\t"
  "v_mul_f32_e32 v36, v36, v241\n\t"
  "v_mul_f32_e32 v37, v37, v241\n\t"
  "v_mul_f32_e32 v38, v38, v241\n\t"
  "v_mul_f32_e32 v39, v39, v241\n\t"
  "v_mul_f32_e32 v40, v40, v240\n\t"
  "v_mul_f32_e32 v41, v41, v240\n\t"
  "v_mul_f32_e32 v42, v42, v240\n\t"
  "v_mul_f32_e32 v43, v43, v240\n\t"
  "v_mul_f32_e32 v44, v44, v241\n\t"
  "v_mul_f32_e32 v45, v45, v241\n\t"
  "v_mul_f32_e32 v46, v46, v241\n\t"
  "v_mul_f32_e32 v47, v47, v241\n\t"
  "v_mul_f32_e32 v48, v48, v240\n\t"
  "v_mul_f32_e32 v49, v49, v240\n\t"
  "v_mul_f32_e32 v50, v50, v240\n\t"
  "v_mul_f32_e32 v51, v51, v240\n\t"
  "v_mul_f32_e32 v52, v52, v241\n\t"
  "v_mul_f32_e32 v53, v53, v241\n\t"
  "v_mul_f32_e32 v54, v54, v241\n\t"
  "v_mul_f32_e32 v55, v55, v241\n\t"
  "v_mul_f32_e32 v56, v56, v240\n\t"
  "v_mul_f32_e32 v57, v57, v240\n\t"
  "v_mul_f32_e32 v58, v58, v240\n\t"
  "v_mul_f32_e32 v59, v59, v240\n\t"
  "v_mul_f32_e32 v60, v60, v241\n\t"
  "v_mul_f32_e32 v61, v61, v241\n\t"
  "v_mul_f32_e32 v62, v62, v241\n\t"
  "v_mul_f32_e32 v63, v63, v241\n\t"
  "v_mul_f32_e32 v236, v236, v240\n\t"
  "v_mul_f32_e32 v237, v237, v241\n\t"
  "v_exp_f32_e32 v228, v96\n\t"
  "v_exp_f32_e32 v229, v97\n\t"
  "v_exp_f32_e32 v230, v98\n\t"
  "v_exp_f32_e32 v231, v99\n\t"
  "v_cvt_pk_bf16_f32 v148, v228, v229\n\t"
  "v_exp_f32_e32 v232, v100\n\t"
  "v_exp_f32_e32 v233, v101\n\t"
  "v_cvt_pk_bf16_f32 v149, v230, v231\n\t"
  "v_exp_f32_e32 v234, v102\n\t"
  "v_exp_f32_e32 v235, v103\n\t"
  "v_cvt_pk_bf16_f32 v152, v232, v233\n\t"
  "v_exp_f32_e32 v228, v104\n\t"
  "v_exp_f32_e32 v229, v105\n\t"
  "v_cvt_pk_bf16_f32 v153, v234, v235\n\t"
  "v_exp_f32_e32 v230, v106\n\t"
  "v_exp_f32_e32 v231, v107\n\t"
  "v_cvt_pk_bf16_f32 v150, v228, v229\n\t"
  "v_exp_f32_e32 v232, v108\n\t"
  "v_exp_f32_e32 v233, v109\n\t"
  "v_cvt_pk_bf16_f32 v151, v230, v231\n\t"
  "v_exp_f32_e32 v234, v110\n\t"
  "v_exp_f32_e32 v235, v111\n\t"
  "v_cvt_pk_bf16_f32 v154, v232, v233\n\t"
  "s_nop 0\n\t"
  "v_cvt_pk_bf16_f32 v155, v234, v235\n\t"
  "s_nop 1\n\t"
  "v_mfma_f32_16x16x32_bf16 v[140:143], v[136:139], v[148:151], 0\n\t"
  "v_mfma_f32_16x16x32_bf16 v[144:147], v[136:139], v[152:155], 0\n\t"
  "v_exp_f32_e32 v228, v112\n\t"
  "v_exp_f32_e32 v229, v113\n\t"
  "v_exp_f32_e32 v230, v114\n\t"
  "v_exp_f32_e32 v231, v115\n\t"
  "s_nop 7\n\t"
  "s_branch .Lslow2_%=\n\t"
  ".Lrare2_%=:\n\t"
  "s_nop 7\n\t"
  "v_max3_f32 v238, v112, v113, v114\n\t"
  "v_max3_f32 v239, v116, v117, v118\n\t"
  "v_max3_f32 v238, v238, v115, v120\n\t"
  "v_max3_f32 v239, v239, v119, v124\n\t"
  "v_max3_f32 v238, v238, v121, v122\n\t"
  "v_max3_f32 v239, v239, v125, v126\n\t"
  "v_max_f32_e32 v238, v238, v123\n\t"
  "v_max_f32_e32 v239, v239, v127\n\t"
  "v_mov_b32_e32 v240, v238\n\t"
  "v_mov_b32_e32 v241, v239\n\t"
  "s_nop 0\n\t"
  "v_permlane16_swap_b32_e32 v238, v240\n\t"
  "v_permlane16_swap_b32_e32 v239, v241\n\t"
  "v_max_f32_e32 v238, v238, v240\n\t"
  "v_max_f32_e32 v239, v239, v241\n\t"
  "v_mov_b32_e32 v240, v238\n\t"
  "v_mov_b32_e32 v241, v239\n\t"
  "s_nop 0\n\t"
  "v_permlane32_swap_b32_e32 v238, v240\n\t"
  "v_permlane32_swap_b32_e32 v239, v241\n\t"
  "v_max3_f32 v238, v238, v240, 0\n\t"
  "v_max3_f32 v239, v239, v241, 0\n\t"
  "v_exp_f32_e64 v240, -v238\n\t"
  "v_exp_f32_e64 v241, -v239\n\t"
  "v_sub_f32_e32 v112, v112, v238\n\t"
  "v_sub_f32_e32 v113, v113, v238\n\t"
  "v_sub_f32_e32 v114, v114, v238\n\t"
  "v_sub_f32_e32 v115, v115, v238\n\t"
  "v_sub_f32_e32 v120, v120, v238\n\t"
  "v_sub_f32_e32 v121, v121, v238\n\t"
  "v_sub_f32_e32 v122, v122, v238\n\t"
  "v_sub_f32_e32 v123, v123, v238\n\t"
  "v_sub_f32_e32 v116, v116, v239\n\t"
  "v_sub_f32_e32 v117, v117, v239\n\t"
  "v_sub_f32_e32 v118, v118, v239\n\t"
  "v_sub_f32_e32 v119, v119, v239\n\t"
  "v_sub_f32_e32 v124, v124, v239\n\t"
  "v_sub_f32_e32 v125, v125, v239\n\t"
  "v_sub_f32_e32 v126, v126, v239\n\t"
  "v_sub_f32_e32 v127, v127, v239\n\t"
  "v_sub_f32_e32 v96, v96, v238\n\t"
  "v_sub_f32_e32 v97, v97, v238\n\t"
  "v_sub_f32_e32 v98, v98, v238\n\t"
  "v_sub_f32_e32 v99, v99, v238\n\t"
  "v_sub_f32_e32 v104, v104, v238\n\t"
  "v_sub_f32_e32 v105, v105, v238\n\t"
  "v_sub_f32_e32 v106, v106, v238\n\t"
  "v_sub_f32_e32 v107, v107, v238\n\t"
  "v_sub_f32_e32 v100, v100, v239\n\t"
  "v_sub_f32_e32 v101, v101, v239\n\t"
  "v_sub_f32_e32 v102, v102, v239\n\t"
  "v_sub_f32_e32 v103, v103, v239\n\t"
  "v_sub_f32_e32 v108, v108, v239\n\t"
  "v_sub_f32_e32 v109, v109, v239\n\t"
  "v_sub_f32_e32 v110, v110, v239\n\t"
  "v_sub_f32_e32 v111, v111, v239\n\t"
  "v_sub_f32_e32 v128, v128, v238\n\t"
  "v_sub_f32_e32 v129, v129, v238\n\t"
  "v_sub_f32_e32 v130, v130, v238\n\t"
  "v_sub_f32_e32 v131, v131, v238\n\t"
  "v_sub_f32_e32 v132, v132, v239\n\t"
  "v_sub_f32_e32 v133, v133, v239\n\t"
  "v_sub_f32_e32 v134, v134, v239\n\t"
  "v_sub_f32_e32 v135, v135, v239\n\t"
  "v_mul_f32_e32 v0, v0, v240\n\t"
  "v_mul_f32_e32 v1, v1, v240\n\t"
  "v_mul_f32_e32 v2, v2, v240\n\t"
  "v_mul_f32_e32 v3, v3, v240\n\t"
  "v_mul_f32_e32 v4, v4, v241\n\t"
  "v_mul_f32_e32 v5, v5, v241\n\t"
  "v_mul_f32_e32 v6, v6, v241\n\t"
  "v_mul_f32_e32 v7, v7, v241\n\t"
  "v_mul_f32_e32 v8, v8, v240\n\t"
  "v_mul_f32_e32 v9, v9, v240\n\t"
  "v_mul_f32_e32 v10, v10, v240\n\t"
  "v_mul_f32_e32 v11, v11, v240\n\t"
  "v_mul_f32_e32 v12, v12, v241\n\t"
  "v_mul_f32_e32 v13, v13, v241\n\t"
  "v_mul_f32_e32 v14, v14, v241\n\t"
  "v_mul_f32_e32 v15, v15, v241\n\t"
  "v_mul_f32_e32 v16, v16, v240\n\t"
  "v_mul_f32_e32 v17, v17, v240\n\t"
  "v_mul_f32_e32 v18, v18, v240\n\t"
  "v_mul_f32_e32 v19, v19, v240\n\t"
  "v_mul_f32_e32 v20, v20, v241\n\t"
  "v_mul_f32_e32 v21, v21, v241\n\t"
  "v_mul_f32_e32 v22, v22, v241\n\t"
  "v_mul_f32_e32 v23, v23, v241\n\t"
  "v_mul_f32_e32 v24, v24, v240\n\t"
  "v_mul_f32_e32 v25, v25, v240\n\t"
  "v_mul_f32_e32 v26, v26, v240\n\t"
  "v_mul_f32_e32 v27, v27, v240\n\t"
  "v_mul_f32_e32 v28, v28, v241\n\t"
  "v_mul_f32_e32 v29, v29, v241\n\t"
  "v_mul_f32_e32 v30, v30, v241\n\t"
  "v_mul_f32_e32 v31, v31, v241\n\t"
  "v_mul_f32_e32 v32, v32, v240\n\t"
  "v_mul_f32_e32 v33, v33, v240\n\t"
  "v_mul_f32_e32 v34, v34, v240\n\t"
  "v_mul_f32_e32 v35, v35, v240\n\t"
  "v_mul_f32_e32 v36, v36, v241\n\t"
  "v_mul_f32_e32 v37, v37, v241\n\t"
  "v_mul_f32_e32 v38, v38, v241\n\t"
  "v_mul_f32_e32 v39, v39, v241\n\t"
  "v_mul_f32_e32 v40, v40, v240\n\t"
  "v_mul_f32_e32 v41, v41, v240\n\t"
  "v_mul_f32_e32 v42, v42, v240\n\t"
  "v_mul_f32_e32 v43, v43, v240\n\t"
  "v_mul_f32_e32 v44, v44, v241\n\t"
  "v_mul_f32_e32 v45, v45, v241\n\t"
  "v_mul_f32_e32 v46, v46, v241\n\t"
  "v_mul_f32_e32 v47, v47, v241\n\t"
  "v_mul_f32_e32 v48, v48, v240\n\t"
  "v_mul_f32_e32 v49, v49, v240\n\t"
  "v_mul_f32_e32 v50, v50, v240\n\t"
  "v_mul_f32_e32 v51, v51, v240\n\t"
  "v_mul_f32_e32 v52, v52, v241\n\t"
  "v_mul_f32_e32 v53, v53, v241\n\t"
  "v_mul_f32_e32 v54, v54, v241\n\t"
  "v_mul_f32_e32 v55, v55, v241\n\t"
  "v_mul_f32_e32 v56, v56, v240\n\t"
  "v_mul_f32_e32 v57, v57, v240\n\t"
  "v_mul_f32_e32 v58, v58, v240\n\t"
  "v_mul_f32_e32 v59, v59, v240\n\t"
  "v_mul_f32_e32 v60, v60, v241\n\t"
  "v_mul_f32_e32 v61, v61, v241\n\t"
  "v_mul_f32_e32 v62, v62, v241\n\t"
  "v_mul_f32_e32 v63, v63, v241\n\t"
  "v_mul_f32_e32 v236, v236, v240\n\t"
  "v_mul_f32_e32 v237, v237, v241\n\t"
  "v_exp_f32_e32 v228, v112\n\t"
  "v_exp_f32_e32 v229, v113\n\t"
  "v_exp_f32_e32 v230, v114\n\t"
  "v_exp_f32_e32 v231, v115\n\t"
  "v_cvt_pk_bf16_f32 v156, v228, v229\n\t"
  "v_exp_f32_e32 v232, v116\n\t"
  "v_exp_f32_e32 v233, v117\n\t"
  "v_cvt_pk_bf16_f32 v157, v230, v231\n\t"
  "v_exp_f32_e32 v234, v118\n\t"
  "v_exp_f32_e32 v235, v119\n\t"
  "v_cvt_pk_bf16_f32 v160, v232, v233\n\t"
  "v_exp_f32_e32 v228, v120\n\t"
  "v_exp_f32_e32 v229, v121\n\t"
  "v_cvt_pk_bf16_f32 v161, v234, v235\n\t"
  "v_exp_f32_e32 v230, v122\n\t"
  "v_exp_f32_e32 v231, v123\n\t"
  "v_cvt_pk_bf16_f32 v158, v228, v229\n\t"
  "v_exp_f32_e32 v232, v124\n\t"
  "v_exp_f32_e32 v233, v125\n\t"
  "v_cvt_pk_bf16_f32 v159, v230, v231\n\t"
  "v_exp_f32_e32 v234, v126\n\t"
  "v_exp_f32_e32 v235, v127\n\t"
  "v_cvt_pk_bf16_f32 v162, v232, v233\n\t"
  "s_nop 0\n\t"
  "v_cvt_pk_bf16_f32 v163, v234, v235\n\t"
  "s_nop 1\n\t"
  "v_mfma_f32_16x16x32_bf16 v[140:143], v[136:139], v[156:159], 0\n\t"
  "v_mfma_f32_16x16x32_bf16 v[144:147], v[136:139], v[160:163], 0\n\t"
  "s_nop 7\n\t"
  "s_branch .Lret2_%=\n\t"
  ".Lhrare0_%=:\n\t"
  "v_max3_f32 v182, v112, v113, v114\n\t"
  "v_max3_f32 v182, v182, v115, v116\n\t"
  "v_max3_f32 v182, v182, v117, v118\n\t"
  "v_max_f32_e32 v182, v182, v119\n\t"
  "v_mov_b32_e32 v183, v182\n\t"
  "s_nop 1\n\t"
  "v_permlane16_swap_b32_e32 v182, v183\n\t"
  "v_max_f32_e32 v182, v182, v183\n\t"
  "v_mov_b32_e32 v183, v182\n\t"
  "s_nop 1\n\t"
  "v_permlane32_swap_b32_e32 v182, v183\n\t"
  "v_max3_f32 v182, v182, v183, 0\n\t"
  "v_sub_f32_e32 v112, v112, v182\n\t"
  "v_sub_f32_e32 v113, v113, v182\n\t"
  "v_sub_f32_e32 v114, v114, v182\n\t"
  "v_sub_f32_e32 v115, v115, v182\n\t"
  "v_sub_f32_e32 v116, v116, v182\n\t"
  "v_sub_f32_e32 v117, v117, v182\n\t"
  "v_sub_f32_e32 v118, v118, v182\n\t"
  "v_sub_f32_e32 v119, v119, v182\n\t"
  "v_sub_f32_e32 v120, v120, v182\n\t"
  "v_sub_f32_e32 v121, v121, v182\n\t"
  "v_sub_f32_e32 v122, v122, v182\n\t"
  "v_sub_f32_e32 v123, v123, v182\n\t"
  "v_exp_f32_e64 v183, -v182\n\t"
  "s_nop 0\n\t"
  "v_mul_f32_e32 v80, v80, v183\n\t"
  "v_mul_f32_e32 v81, v81, v183\n\t"
  "v_mul_f32_e32 v82, v82, v183\n\t"
  "v_mul_f32_e32 v83, v83, v183\n\t"
  "v_mul_f32_e32 v84, v84, v183\n\t"
  "v_mul_f32_e32 v85, v85, v183\n\t"
  "v_mul_f32_e32 v86, v86, v183\n\t"
  "v_mul_f32_e32 v87, v87, v183\n\t"
  "v_mul_f32_e32 v88, v88, v183\n\t"
  "v_mul_f32_e32 v89, v89, v183\n\t"
  "v_mul_f32_e32 v90, v90, v183\n\t"
  "v_mul_f32_e32 v91, v91, v183\n\t"
  "v_mul_f32_e32 v92, v92, v183\n\t"
  "v_mul_f32_e32 v93, v93, v183\n\t"
  "v_mul_f32_e32 v94, v94, v183\n\t"
  "v_mul_f32_e32 v95, v95, v183\n\t"
  "v_mul_f32_e32 v96, v96, v183\n\t"
  "v_mul_f32_e32 v97, v97, v183\n\t"
  "v_mul_f32_e32 v98, v98, v183\n\t"
  "v_mul_f32_e32 v99, v99, v183\n\t"
  "v_mul_f32_e32 v100, v100, v183\n\t"
  "v_mul_f32_e32 v101, v101, v183\n\t"
  "v_mul_f32_e32 v102, v102, v183\n\t"
  "v_mul_f32_e32 v103, v103, v183\n\t"
  "v_mul_f32_e32 v104, v104, v183\n\t"
  "v_mul_f32_e32 v105, v105, v183\n\t"
  "v_mul_f32_e32 v106, v106, v183\n\t"
  "v_mul_f32_e32 v107, v107, v183\n\t"
  "v_mul_f32_e32 v108, v108, v183\n\t"
  "v_mul_f32_e32 v109, v109, v183\n\t"
  "v_mul_f32_e32 v110, v110, v183\n\t"
  "v_mul_f32_e32 v111, v111, v183\n\t"
  "v_mul_f32_e32 v181, v181, v183\n\t"
  "v_exp_f32_e32 v168, v112\n\t"
  "v_exp_f32_e32 v169, v113\n\t"
  "v_exp_f32_e32 v170, v114\n\t"
  "v_exp_f32_e32 v171, v115\n\t"
  "v_exp_f32_e32 v172, v116\n\t"
  "v_exp_f32_e32 v173, v117\n\t"
  "v_exp_f32_e32 v174, v118\n\t"
  "v_exp_f32_e32 v175, v119\n\t"
  "s_nop 0\n\t"
  "v_cvt_pk_bf16_f32 v132, v168, v169\n\t"
  "v_cvt_pk_bf16_f32 v133, v170, v171\n\t"
  "v_cvt_pk_bf16_f32 v134, v172, v173\n\t"
  "v_cvt_pk_bf16_f32 v135, v174, v175\n\t"
  "s_setprio 3\n\t"
  "s_nop 0\n\t"
  "v_mfma_f32_16x16x32_bf16 v[128:131], v[124:127], v[132:135], 0\n\t"
  "s_setprio 0\n\t"
  "s_nop 7\n\t"
  "s_branch .Lhret0_%=\n\t"
  ".Lhrare1_%=:\n\t"
  "v_max3_f32 v182, v112, v113, v114\n\t"
  "v_max3_f32 v182, v182, v115, v116\n\t"
  "v_max3_f32 v182, v182, v117, v118\n\t"
  "v_max_f32_e32 v182, v182, v119\n\t"
  "v_mov_b32_e32 v183, v182\n\t"
  "s_nop 1\n\t"
  "v_permlane16_swap_b32_e32 v182, v183\n\t"
  "v_max_f32_e32 v182, v182, v183\n\t"
  "v_mov_b32_e32 v183, v182\n\t"
  "s_nop 1\n\t"
  "v_permlane32_swap_b32_e32 v182, v183\n\t"
  "v_max3_f32 v182, v182, v183, 0\n\t"
  "v_sub_f32_e32 v112, v112, v182\n\t"
  "v_sub_f32_e32 v113, v113, v182\n\t"
  "v_sub_f32_e32 v114, v114, v182\n\t"
  "v_sub_f32_e32 v115, v115, v182\n\t"
  "v_sub_f32_e32 v116, v116, v182\n\t"
  "v_sub_f32_e32 v117, v117, v182\n\t"
  "v_sub_f32_e32 v118, v118, v182\n\t"
  "v_sub_f32_e32 v119, v119, v182\n\t"
  "v_sub_f32_e32 v120, v120, v182\n\t"
  "v_sub_f32_e32 v121, v121, v182\n\t"
  "v_sub_f32_e32 v122, v122, v182\n\t"
  "v_sub_f32_e32 v123, v123, v182\n\t"
  "v_exp_f32_e64 v183, -v182\n\t"
  "s_nop 0\n\t"
  "v_mul_f32_e32 v80, v80, v183\n\t"
  "v_mul_f32_e32 v81, v81, v183\n\t"
  "v_mul_f32_e32 v82, v82, v183\n\t"
  "v_mul_f32_e32 v83, v83, v183\n\t"
  "v_mul_f32_e32 v84, v84, v183\n\t"
  "v_mul_f32_e32 v85, v85, v183\n\t"
  "v_mul_f32_e32 v86, v86, v183\n\t"
  "v_mul_f32_e32 v87, v87, v183\n\t"
  "v_mul_f32_e32 v88, v88, v183\n\t"
  "v_mul_f32_e32 v89, v89, v183\n\t"
  "v_mul_f32_e32 v90, v90, v183\n\t"
  "v_mul_f32_e32 v91, v91, v183\n\t"
  "v_mul_f32_e32 v92, v92, v183\n\t"
  "v_mul_f32_e32 v93, v93, v183\n\t"
  "v_mul_f32_e32 v94, v94, v183\n\t"
  "v_mul_f32_e32 v95, v95, v183\n\t"
  "v_mul_f32_e32 v96, v96, v183\n\t"
  "v_mul_f32_e32 v97, v97, v183\n\t"
  "v_mul_f32_e32 v98, v98, v183\n\t"
  "v_mul_f32_e32 v99, v99, v183\n\t"
  "v_mul_f32_e32 v100, v100, v183\n\t"
  "v_mul_f32_e32 v101, v101, v183\n\t"
  "v_mul_f32_e32 v102, v102, v183\n\t"
  "v_mul_f32_e32 v103, v103, v183\n\t"
  "v_mul_f32_e32 v104, v104, v183\n\t"
  "v_mul_f32_e32 v105, v105, v183\n\t"
  "v_mul_f32_e32 v106, v106, v183\n\t"
  "v_mul_f32_e32 v107, v107, v183\n\t"
  "v_mul_f32_e32 v108, v108, v183\n\t"
  "v_mul_f32_e32 v109, v109, v183\n\t"
  "v_mul_f32_e32 v110, v110, v183\n\t"
  "v_mul_f32_e32 v111, v111, v183\n\t"
  "v_mul_f32_e32 v181, v181, v183\n\t"
  "v_exp_f32_e32 v168, v112\n\t"
  "v_exp_f32_e32 v169, v113\n\t"
  "v_exp_f32_e32 v170, v114\n\t"
  "v_exp_f32_e32 v171, v115\n\t"
  "v_exp_f32_e32 v172, v116\n\t"
  "v_exp_f32_e32 v173, v117\n\t"
  "v_exp_f32_e32 v174, v118\n\t"
  "v_exp_f32_e32 v175, v119\n\t"
  "s_nop 0\n\t"
  "v_cvt_pk_bf16_f32 v132, v168, v169\n\t"
  "v_cvt_pk_bf16_f32 v133, v170, v171\n\t"
  "v_cvt_pk_bf16_f32 v134, v172, v173\n\t"
  "v_cvt_pk_bf16_f32 v135, v174, v175\n\t"
  "s_setprio 3\n\t"
  "s_nop 0\n\t"
  "v_mfma_f32_16x16x32_bf16 v[128:131], v[124:127], v[132:135], 0\n\t"
  "s_setprio 0\n\t"
  "s_nop 7\n\t"
  "s_branch .Lhret1_%=\n\t"
  ".Lhrare2_%=:\n\t"
  "v_max3_f32 v182, v112, v113, v114\n\t"
  "v_max3_f32 v182, v182, v115, v116\n\t"
  "v_max3_f32 v182, v182, v117, v118\n\t"
  "v_max_f32_e32 v182, v182, v119\n\t"
  "v_mov_b32_e32 v183, v182\n\t"
  "s_nop 1\n\t"
  "v_permlane16_swap_b32_e32 v182, v183\n\t"
  "v_max_f32_e32 v182, v182, v183\n\t"
  "v_mov_b32_e32 v183, v182\n\t"
  "s_nop 1\n\t"
  "v_permlane32_swap_b32_e32 v182, v183\n\t"
  "v_max3_f32 v182, v182, v183, 0\n\t"
  "v_sub_f32_e32 v112, v112, v182\n\t"
  "v_sub_f32_e32 v113, v113, v182\n\t"
  "v_sub_f32_e32 v114, v114, v182\n\t"
  "v_sub_f32_e32 v115, v115, v182\n\t"
  "v_sub_f32_e32 v116, v116, v182\n\t"
  "v_sub_f32_e32 v117, v117, v182\n\t"
  "v_sub_f32_e32 v118, v118, v182\n\t"
  "v_sub_f32_e32 v119, v119, v182\n\t"
  "v_sub_f32_e32 v120, v120, v182\n\t"
  "v_sub_f32_e32 v121, v121, v182\n\t"
  "v_sub_f32_e32 v122, v122, v182\n\t"
  "v_sub_f32_e32 v123, v123, v182\n\t"
  "v_exp_f32_e64 v183, -v182\n\t"
  "s_nop 0\n\t"
  "v_mul_f32_e32 v80, v80, v183\n\t"
  "v_mul_f32_e32 v81, v81, v183\n\t"
  "v_mul_f32_e32 v82, v82, v183\n\t"
  "v_mul_f32_e32 v83, v83, v183\n\t"
  "v_mul_f32_e32 v84, v84, v183\n\t"
  "v_mul_f32_e32 v85, v85, v183\n\t"
  "v_mul_f32_e32 v86, v86, v183\n\t"
  "v_mul_f32_e32 v87, v87, v183\n\t"
  "v_mul_f32_e32 v88, v88, v183\n\t"
  "v_mul_f32_e32 v89, v89, v183\n\t"
  "v_mul_f32_e32 v90, v90, v183\n\t"
  "v_mul_f32_e32 v91, v91, v183\n\t"
  "v_mul_f32_e32 v92, v92, v183\n\t"
  "v_mul_f32_e32 v93, v93, v183\n\t"
  "v_mul_f32_e32 v94, v94, v183\n\t"
  "v_mul_f32_e32 v95, v95, v183\n\t"
  "v_mul_f32_e32 v96, v96, v183\n\t"
  "v_mul_f32_e32 v97, v97, v183\n\t"
  "v_mul_f32_e32 v98, v98, v183\n\t"
  "v_mul_f32_e32 v99, v99, v183\n\t"
  "v_mul_f32_e32 v100, v100, v183\n\t"
  "v_mul_f32_e32 v101, v101, v183\n\t"
  "v_mul_f32_e32 v102, v102, v183\n\t"
  "v_mul_f32_e32 v103, v103, v183\n\t"
  "v_mul_f32_e32 v104, v104, v183\n\t"
  "v_mul_f32_e32 v105, v105, v183\n\t"
  "v_mul_f32_e32 v106, v106, v183\n\t"
  "v_mul_f32_e32 v107, v107, v183\n\t"
  "v_mul_f32_e32 v108, v108, v183\n\t"
  "v_mul_f32_e32 v109, v109, v183\n\t"
  "v_mul_f32_e32 v110, v110, v183\n\t"
  "v_mul_f32_e32 v111, v111, v183\n\t"
  "v_mul_f32_e32 v181, v181, v183\n\t"
  "v_exp_f32_e32 v168, v112\n\t"
  "v_exp_f32_e32 v169, v113\n\t"
  "v_exp_f32_e32 v170, v114\n\t"
  "v_exp_f32_e32 v171, v115\n\t"
  "v_exp_f32_e32 v172, v116\n\t"
  "v_exp_f32_e32 v173, v117\n\t"
  "v_exp_f32_e32 v174, v118\n\t"
  "v_exp_f32_e32 v175, v119\n\t"
  "s_nop 0\n\t"
  "v_cvt_pk_bf16_f32 v132, v168, v169\n\t"
  "v_cvt_pk_bf16_f32 v133, v170, v171\n\t"
  "v_cvt_pk_bf16_f32 v134, v172, v173\n\t"
  "v_cvt_pk_bf16_f32 v135, v174, v175\n\t"
  "s_setprio 3\n\t"
  "s_nop 0\n\t"
  "v_mfma_f32_16x16x32_bf16 v[128:131], v[124:127], v[132:135], 0\n\t"
  "s_setprio 0\n\t"
  "s_nop 7\n\t"
  "s_branch .Lhret2_%=\n\t"
    :: "s"(__builtin_amdgcn_kernarg_segment_ptr()), "s"(blockIdx.x), "s"(wid), "s"(ldsb) : "v0", "v1", "v2", "v3", "v4", "v5", "v6", "v7", "v8", "v9", "v10", "v11", "v12", "v13", "v14", "v15", "v16", "v17", "v18", "v19", "v20", "v21", "v22", "v23", "v24", "v25", "v26", "v27", "v28", "v29", "v30", "v31", "v32", "v33", "v34", "v35", "v36", "v37", "v38", "v39", "v40", "v41", "v42", "v43", "v44", "v45", "v46", "v47", "v48", "v49", "v50", "v51", "v52", "v53", "v54", "v55", "v56", "v57", "v58", "v59", "v60", "v61", "v62", "v63", "v64", "v65", "v66", "v67", "v68", "v69", "v70", "v71", "v72", "v73", "v74", "v75", "v76", "v77", "v78", "v79", "v80", "v81", "v82", "v83", "v84", "v85", "v86", "v87", "v88", "v89", "v90", "v91", "v92", "v93", "v94", "v95", "v96", "v97", "v98", "v99", "v100", "v101", "v102", "v103", "v104", "v105", "v106", "v107", "v108", "v109", "v110", "v111", "v112", "v113", "v114", "v115", "v116", "v117", "v118", "v119", "v120", "v121", "v122", "v123", "v124", "v125", "v126", "v127", "v128", "v129", "v130", "v131", "v132", "v133", "v134", "v135", "v136", "v137", "v138", "v139", "v140", "v141", "v142", "v143", "v144", "v145", "v146", "v147", "v148", "v149", "v150", "v151", "v152", "v153", "v154", "v155", "v156", "v157", "v158", "v159", "v160", "v161", "v162", "v163", "v164", "v165", "v166", "v167", "v168", "v169", "v170", "v171", "v172", "v173", "v174", "v175", "v176", "v177", "v178", "v179", "v180", "v181", "v182", "v183", "v184", "v185", "v186", "v187", "v188", "v189", "v190", "v191", "v192", "v193", "v194", "v195", "v196", "v197", "v198", "v199", "v200", "v201", "v202", "v203", "v204", "v205", "v206", "v207", "v208", "v209", "v210", "v211", "v212", "v213", "v214", "v215", "v216", "v217", "v218", "v219", "v220", "v221", "v222", "v223", "v224", "v225", "v226", "v227", "v228", "v229", "v230", "v231", "v232", "v233", "v234", "v235", "v236", "v237", "v238", "v239", "v240", "v241", "v242", "v243", "v244", "v245", "v246", "v247", "v248", "v249", "v250", "v251", "v252", "v253", "v254", "v255", "s36", "s37", "s38", "s39", "s40", "s41", "s42", "s43", "s44", "s45", "s46", "s47", "s48", "s49", "s50", "s51", "s52", "s53", "s54", "s55", "s56", "s57", "s58", "s59", "s60", "s61", "s62", "s63", "s64", "s65", "s66", "s67", "s68", "s69", "s70", "s71", "s72", "s73", "s74", "s75", "s76", "s77", "s78", "s79", "s80", "s81", "s82", "s83", "s84", "s85", "s86", "s87", "s88", "s89", "s90", "s91", "s92", "s93", "s94", "s95", "s96", "s97", "s98", "s99", "vcc", "scc", "memory");
}

extern "C" void kernel_launch(void* const* d_in, const int* in_sizes, int n_in, void* d_out, int out_size, void* d_ws, size_t ws_size,
                              hipStream_t stream) {
  hipLaunchKernelGGL(gqa_fwd, dim3(256), dim3(512), 0, stream, (const void*)d_in[0], (const void*)d_in[1], (const void*)d_in[2], d_out);
}
```
